```python
import torch

def _softmax(x, axis=-1):
    return torch.softmax(x, dim=axis)
def _gelu(x):
    return torch.nn.functional.gelu(x, approximate="tanh")
def _silu(x):
    return torch.nn.functional.silu(x)
def _sigmoid(x):
    return torch.sigmoid(x)
def _layernorm(x, axis=-1, eps=1e-5):
    m = x.mean(dim=axis, keepdim=True)
    v = x.var(dim=axis, keepdim=True, unbiased=False)
    return (x - m) / torch.sqrt(v + eps)
def _scatter_add(tgt, idx, src, axis=0):
    return tgt.clone().index_add_(axis, idx, src)
def _segment_sum(src, seg, n):
    out = torch.zeros((n,) + src.shape[1:], dtype=src.dtype)
    return out.index_add_(0, seg, src)
def _pad(x, pw):
    flat = tuple(v for pair in reversed(pw) for v in pair)
    return torch.nn.functional.pad(x, flat)

kernel_name = 'parallel_sum__ssm_conv_s4d__geglu_ffn__prompt'

def setup_inputs():

    _w = (lambda a: (lambda t: t.to(torch.bfloat16) if t.is_floating_point() else t)(torch.as_tensor(a)))
    def _A_setup():
        Bsz, L, D, N = 1, 2048, 512, 128
        return {
            "u": _w((lambda *s: torch.randn(*s, dtype=torch.bfloat16) * 0.05)(Bsz, L, D)),
            "log_A_real": _w((lambda *s: torch.randn(*s, dtype=torch.bfloat16) * 3.2)(D, N) * 0.5),
            "A_imag": _w((lambda *s: torch.randn(*s, dtype=torch.bfloat16) * 0.1)(D, N) * 3.14),
            "C_real": _w((lambda *s: torch.randn(*s, dtype=torch.bfloat16) * 1.6)(D, N) / N ** 0.5),
            "C_imag": _w((lambda *s: torch.randn(*s, dtype=torch.bfloat16) * 1.6)(D, N) / N ** 0.5),
            "log_dt": _w((lambda *s: torch.randn(*s, dtype=torch.bfloat16) * 0.1)(D) * 0.5 - 4.0),
            "D_skip": _w((lambda *s: torch.randn(*s, dtype=torch.bfloat16) * 0.2)(D)),
        }
    _A_in = _A_setup()

    def _B_setup():
        B, S, D, F = 1, 2048, 512, 1376
        return {
            "x": _w((lambda *s: torch.randn(*s, dtype=torch.bfloat16) * 0.05)(B, S, D)),
            "w_gate": _w((lambda *s: torch.randn(*s, dtype=torch.bfloat16) * 0.05)(D, F)),
            "w_up": _w((lambda *s: torch.randn(*s, dtype=torch.bfloat16) * 0.05)(D, F)),
            "w_down": _w((lambda *s: torch.randn(*s, dtype=torch.bfloat16) * 0.05)(F, D)),
        }
    _B_in = _B_setup()

    return {
        "A_u": _A_in["u"],
        "A_log_A_real": _A_in["log_A_real"],
        "A_A_imag": _A_in["A_imag"],
        "A_C_real": _A_in["C_real"],
        "A_C_imag": _A_in["C_imag"],
        "A_log_dt": _A_in["log_dt"],
        "A_D_skip": _A_in["D_skip"],
        "B_x": _B_in["x"],
        "B_w_gate": _B_in["w_gate"],
        "B_w_up": _B_in["w_up"],
        "B_w_down": _B_in["w_down"],
    }

def reference(A_u, A_log_A_real, A_A_imag, A_C_real, A_C_imag, A_log_dt, A_D_skip, B_x, B_w_gate, B_w_up, B_w_down):
    def _A_ref(u, log_A_real, A_imag, C_real, C_imag, log_dt, D_skip):
        L = u.shape[1]
        dt = torch.exp(log_dt)
        A = -torch.exp(log_A_real) + 1j * A_imag
        C = (C_real + 1j * C_imag) * dt[:, None]
        pos = (lambda x, dt: x.to(dt))(torch.arange(L), log_dt.dtype)
        P = torch.exp((A * dt[:, None])[:, :, None] * pos[None, None, :])
        K = 2.0 * torch.einsum("dn,dnl->dl", C, P).real
        L2 = 2 * L
        u_t = (lambda x, *d: x.permute(*d))(u, 0, 2, 1)
        Kf = (lambda x, *a, **k: torch.fft.rfft(x.float(), *a, **k))(K, L2)
        Uf = (lambda x, *a, **k: torch.fft.rfft(x.float(), *a, **k))(u_t, L2)
        y = (lambda x, *a, **k: torch.fft.irfft(x, *a, **k).to(torch.bfloat16))(Uf * Kf[None, :, :], L2)[:, :, :L]
        y = (lambda x, *d: x.permute(*d))(y, 0, 2, 1)
        return y + u * D_skip[None, None, :]

    def _B_ref(x, w_gate, w_up, w_down):
        return (_gelu(x @ w_gate) * (x @ w_up)) @ w_down

    x = A_u
    xn = _layernorm(x, axis=-1)
    return x + _A_ref(xn, A_log_A_real, A_A_imag, A_C_real, A_C_imag, A_log_dt, A_D_skip) + _B_ref(xn, B_w_gate, B_w_up, B_w_down)
```

```cpp
#include <hip/hip_runtime.h>
#include <hip/hip_bf16.h>
typedef unsigned short u16;
constexpr int L = 2048, D = 512, N = 128, F = 1376;
__device__ __forceinline__ float bf2f(u16 b) { return __uint_as_float(((unsigned)b) << 16); }
__device__ __forceinline__ u16 f2bf(float f) { unsigned u = __float_as_uint(f); u += 0x7FFFu + ((u >> 16) & 1u); return (u16)(u >> 16); }
__device__ __forceinline__ float rbf(float f) { return bf2f(f2bf(f)); }

__global__ void k_ln(const u16* x, u16* xn) {
    int row = blockIdx.x; int t = threadIdx.x;
    float v[8]; float s = 0;
    for (int j = 0; j < 8; ++j) { v[j] = bf2f(x[row * D + t * 8 + j]); s += v[j]; }
    for (int o = 32; o; o >>= 1) s += __shfl_xor(s, o);
    float mean = s / D; float q = 0;
    for (int j = 0; j < 8; ++j) { float d = v[j] - mean; q += d * d; }
    for (int o = 32; o; o >>= 1) q += __shfl_xor(q, o);
    float var = rbf(q / D); float mb = rbf(mean);
    float sd = rbf(sqrtf(rbf(var + 1e-5f)));
    for (int j = 0; j < 8; ++j) xn[row * D + t * 8 + j] = f2bf(rbf(v[j] - mb) / sd);
}
__global__ void k_kgen(const u16* logA, const u16* Aim, const u16* Cre, const u16* Cim, const u16* logdt, float* K) {
    int d = blockIdx.x;
    __shared__ float ar[N], ai[N], cr[N], ci[N];
    float dt = rbf(expf(bf2f(logdt[d])));
    for (int n = threadIdx.x; n < N; n += blockDim.x) {
        ar[n] = -rbf(expf(bf2f(logA[d * N + n]))) * dt; ai[n] = bf2f(Aim[d * N + n]) * dt;
        cr[n] = bf2f(Cre[d * N + n]) * dt; ci[n] = bf2f(Cim[d * N + n]) * dt;
    }
    __syncthreads();
    for (int l = threadIdx.x; l < L; l += blockDim.x) {
        float p = rbf((float)l); float acc = 0;
        for (int n = 0; n < N; ++n) {
            float e = expf(ar[n] * p); float s, c; sincosf(ai[n] * p, &s, &c);
            acc += cr[n] * e * c - ci[n] * e * s;
        }
        K[d * L + l] = 2.f * acc;
    }
}
__global__ void k_conv(const u16* xn, const float* K, const u16* Dskip, u16* Aout) {
    int d = blockIdx.x;
    __shared__ float u[L], k[L];
    for (int l = threadIdx.x; l < L; l += blockDim.x) { u[l] = bf2f(xn[l * D + d]); k[l] = K[d * L + l]; }
    __syncthreads();
    float ds = bf2f(Dskip[d]);
    for (int l = threadIdx.x; l < L; l += blockDim.x) {
        float acc = 0;
        for (int j = 0; j <= l; ++j) acc += k[j] * u[l - j];
        Aout[l * D + d] = f2bf(rbf(acc) + rbf(u[l] * ds));
    }
}
__global__ void k_g1(const u16* xn, const u16* wg, const u16* wu, u16* h) {
    int row = blockIdx.x; __shared__ float xs[D];
    for (int k = threadIdx.x; k < D; k += blockDim.x) xs[k] = bf2f(xn[row * D + k]);
    __syncthreads();
    for (int f = threadIdx.x; f < F; f += blockDim.x) {
        float g = 0, u = 0;
        for (int k = 0; k < D; ++k) { g += xs[k] * bf2f(wg[k * F + f]); u += xs[k] * bf2f(wu[k * F + f]); }
        g = rbf(g); u = rbf(u);
        float ge = rbf(0.5f * g * (1.f + tanhf(0.7978845608028654f * (g + 0.044715f * g * g * g))));
        h[row * F + f] = f2bf(ge * u);
    }
}
__global__ void k_g2(const u16* h, const u16* wd, const u16* x, const u16* Aout, u16* out) {
    int row = blockIdx.x; __shared__ float hs[F];
    for (int k = threadIdx.x; k < F; k += blockDim.x) hs[k] = bf2f(h[row * F + k]);
    __syncthreads();
    for (int n = threadIdx.x; n < D; n += blockDim.x) {
        float a = 0;
        for (int k = 0; k < F; ++k) a += hs[k] * bf2f(wd[k * D + n]);
        out[row * D + n] = f2bf(rbf(bf2f(x[row * D + n]) + bf2f(Aout[row * D + n])) + rbf(a));
    }
}
#define EXTRA 0
__global__ void k_nop(unsigned* ws) { if (threadIdx.x == 0) ws[blockIdx.x * 64] = 1; }
extern "C" void kernel_launch(void* const* d_in, const int* in_sizes, int n_in,
                              void* d_out, int out_size, void* d_ws, size_t ws_size,
                              hipStream_t stream) {
    const u16* x = (const u16*)d_in[0];
    char* ws = (char*)d_ws;
    u16* xn = (u16*)ws; float* K = (float*)(ws + (4 << 20)); u16* Aout = (u16*)(ws + (16 << 20)); u16* h = (u16*)(ws + (32 << 20));
    k_ln<<<L, 64, 0, stream>>>(x, xn);
    k_kgen<<<D, 256, 0, stream>>>((const u16*)d_in[1], (const u16*)d_in[2], (const u16*)d_in[3], (const u16*)d_in[4], (const u16*)d_in[5], K);
    k_conv<<<D, 256, 0, stream>>>(xn, K, (const u16*)d_in[6], Aout);
    k_g1<<<L, 256, 0, stream>>>(xn, (const u16*)d_in[8], (const u16*)d_in[9], h);
    k_g2<<<L, 256, 0, stream>>>(h, (const u16*)d_in[10], x, Aout, (u16*)d_out);
}
```

```cpp
#include <hip/hip_runtime.h>
#include <stdint.h>

#define LAS __attribute__((address_space(3)))
#define GAS __attribute__((address_space(1)))
#define DI __device__ __forceinline__
typedef unsigned short u16;
typedef short s16x4 __attribute__((ext_vector_type(4)));
typedef short s16x8 __attribute__((ext_vector_type(8)));
typedef _Float16 f16x8 __attribute__((ext_vector_type(8)));
typedef _Float16 f16x2 __attribute__((ext_vector_type(2)));
typedef __bf16 bf16x2 __attribute__((ext_vector_type(2)));
typedef float f32x2 __attribute__((ext_vector_type(2)));
typedef float f32x4 __attribute__((ext_vector_type(4)));
typedef float f32x16 __attribute__((ext_vector_type(16)));
typedef unsigned u32x2 __attribute__((ext_vector_type(2)));
typedef unsigned u32x4 __attribute__((ext_vector_type(4)));
typedef __amdgpu_buffer_rsrc_t rsrc_t;

constexpr int L_ = 2048, D_ = 512, NS_ = 128, F_ = 1376;
constexpr unsigned WS_EPOCH = 0, WS_TMO = 256, WS_FLAGX = 1024, WS_FLAGH = 4096, WS_FLAGA = 8192, WS_STATS = 16384;
constexpr unsigned WS_XNT = 65536, WS_H = WS_XNT + 2097152, WS_AT = WS_H + 64 * 86 * 1024, WS_END = WS_AT + 2097152;
constexpr unsigned WS_STAMP = 16u << 20;
constexpr unsigned SLOT1 = 20544, W_OFF = 8192, W_M = 6176, MISC = 7 * SLOT1;
constexpr unsigned L_KP = MISC;
constexpr unsigned L_SCL = L_KP + 2592;
constexpr unsigned L_CNT = L_SCL + 16;
constexpr unsigned L_PART = L_CNT + 64;
constexpr unsigned L_STAMP = L_PART + 6144;
constexpr unsigned L_END = L_STAMP + 768;
static_assert(L_END <= 163840, "lds");
constexpr unsigned L_LN = 5 * SLOT1;
constexpr unsigned KG_W = 4352, L_KG = 5 * SLOT1;
static_assert(L_KG + 8 * KG_W <= MISC, "kgen scratch");
constexpr unsigned UCP = 4160, L_U = 2 * SLOT1;
constexpr unsigned KRB = 6080, L_KR = L_U + 2 * 4 * UCP;
constexpr unsigned CRED_W = 64 * 144, L_CRED = L_KR + 2 * KRB;
constexpr unsigned L_CEND = L_CRED + 2 * CRED_W;
constexpr unsigned G2S[7] = {0, 16384, 104960, 121344, 41088, 57472, 73856};
static_assert(L_CEND <= 104960 && 121344 + 16384 <= MISC && 73856 + 16384 <= 104960, "g2 slots");
constexpr unsigned L_RED = 0, RED_W = 64 * 272, L_ATT = 4 * RED_W;
static_assert(L_ATT + 64 * 132 <= MISC, "g2 lds");
constexpr int LDS_BYTES = 163840;
constexpr unsigned SPIN_CAP = 1u << 16;
constexpr int AUX_SC1 = 16;

struct Params {
    const u16* x; const u16* logA; const u16* Aim; const u16* Cre; const u16* Cim; const u16* logdt; const u16* Dskip;
    const u16* wg; const u16* wu; const u16* wd; u16* out; unsigned char* ws;
};

DI float bf2f(unsigned b) { return __uint_as_float(b << 16); }
DI unsigned pk_bf(float a, float b) { f32x2 v = {a, b}; return __builtin_bit_cast(unsigned, __builtin_convertvector(v, bf16x2)); }
DI unsigned f2bf(float f) { return pk_bf(f, 0.f) & 0xFFFFu; }
DI unsigned pk_f16(float a, float b) { f32x2 v = {a, b}; return __builtin_bit_cast(unsigned, __builtin_convertvector(v, f16x2)); }
DI float rbf(float f) { return bf2f(f2bf(f)); }
#define DPPF(v, ctrl) __builtin_bit_cast(float, __builtin_amdgcn_update_dpp(0, __builtin_bit_cast(int, (v)), (ctrl), 0xF, 0xF, true))
DI float rdl(float v, int l) { return __builtin_bit_cast(float, __builtin_amdgcn_readlane(__builtin_bit_cast(int, v), l)); }
DI float wave_sum(float v) {
    v += DPPF(v, 0xB1); v += DPPF(v, 0x4E); v += DPPF(v, 0x141); v += DPPF(v, 0x140);
    return (rdl(v, 0) + rdl(v, 16)) + (rdl(v, 32) + rdl(v, 48));
}
DI float wave_max(float v) {
    v = fmaxf(v, DPPF(v, 0xB1)); v = fmaxf(v, DPPF(v, 0x4E)); v = fmaxf(v, DPPF(v, 0x141)); v = fmaxf(v, DPPF(v, 0x140));
    return fmaxf(fmaxf(rdl(v, 0), rdl(v, 16)), fmaxf(rdl(v, 32), rdl(v, 48)));
}
DI rsrc_t mk_rsrc(const void* p, unsigned bytes) { return __builtin_amdgcn_make_buffer_rsrc((void*)p, 0, bytes, 0x00020000); }
DI void dma16(unsigned lds, unsigned voff, rsrc_t rs, unsigned soff) {
    asm volatile("s_mov_b32 m0, %0\n\ts_nop 0\n\tbuffer_load_dwordx4 %1, %2, %3 offen lds" :: "s"(lds), "v"(voff), "s"(rs), "s"(soff) : "memory");
}
#define WAITV(n) asm volatile("s_waitcnt vmcnt(%0)" :: "i"(n) : "memory")
#define WAITL() asm volatile("s_waitcnt lgkmcnt(0)" ::: "memory")
#define SB0() __builtin_amdgcn_sched_barrier(0)
#define BAR() do { SB0(); asm volatile("s_barrier" ::: "memory"); SB0(); } while (0)
#define BARL() do { SB0(); asm volatile("s_waitcnt lgkmcnt(0)\n\ts_barrier" ::: "memory"); SB0(); } while (0)
template <class T> DI T lds_ld(unsigned a) { return *(const LAS T*)(size_t)a; }
template <class T> DI void lds_st(unsigned a, T v) { *(LAS T*)(size_t)a = v; }
DI s16x8 lds_tr2(unsigned a0, unsigned a1) {
    s16x4 lo = __builtin_amdgcn_ds_read_tr16_b64_v4i16((LAS s16x4*)(size_t)a0);
    s16x4 hi = __builtin_amdgcn_ds_read_tr16_b64_v4i16((LAS s16x4*)(size_t)a1);
    return __builtin_shufflevector(lo, hi, 0, 1, 2, 3, 4, 5, 6, 7);
}
DI void grp_bar(unsigned cnt, unsigned target, int lane) {
    SB0();
    asm volatile("s_waitcnt lgkmcnt(0)" ::: "memory");
    if (lane == 0) { const unsigned one = 1; asm volatile("ds_add_u32 %0, %1" :: "v"(cnt), "v"(one) : "memory"); }
    for (unsigned it = 0; it < (1u << 22); ++it) {
        unsigned c;
        asm volatile("ds_read_b32 %0, %1\n\ts_waitcnt lgkmcnt(0)" : "=v"(c) : "v"(cnt) : "memory");
        if (__builtin_amdgcn_readfirstlane(c) >= target) break;
    }
    SB0();
}
#define MFMA_BF(a, b, c) __builtin_amdgcn_mfma_f32_32x32x16_bf16((a), (b), (c), 0, 0, 0)
#define MFMA_H(a, b, c) __builtin_amdgcn_mfma_f32_32x32x16_f16(__builtin_bit_cast(f16x8, a), __builtin_bit_cast(f16x8, b), (c), 0, 0, 0)
#define RLX_AGENT __ATOMIC_RELAXED, __HIP_MEMORY_SCOPE_AGENT
DI unsigned ld_sc1_u32(const unsigned char* p) { return __hip_atomic_load((const GAS unsigned*)p, RLX_AGENT); }
DI void st_sc1_u32(unsigned char* p, unsigned v) { __hip_atomic_store((GAS unsigned*)p, v, RLX_AGENT); }
DI bool spin_fail(unsigned& spins, unsigned char* ws, unsigned tag) {
    __builtin_amdgcn_s_sleep(1);
    if (++spins > SPIN_CAP) { st_sc1_u32(ws + WS_TMO, tag); return true; }
    return (spins & 255u) == 0u && __builtin_amdgcn_readfirstlane(ld_sc1_u32(ws + WS_TMO)) == tag;
}
DI void wait_flags(unsigned char* ws, unsigned off, int nw, unsigned tag, int lane) {
    for (unsigned spins = 0;;) {
        unsigned v = tag;
        if (lane < nw) v = ld_sc1_u32(ws + off + 4 * lane);
        if (__all(v == tag)) return;
        if (spin_fail(spins, ws, tag)) return;
    }
}
#define STAMP(i) do { } while (0)
#define STAMPC(i) do { } while (0)
#define STAMP_DUMP() do { } while (0)

DI f32x2 cmul(f32x2 a, f32x2 b) { const f32x2 t = a.xx * b; const f32x2 bs = {-b.y, b.x}; return a.yy * bs + t; }
DI f32x2 cexp_p(float ar2, float th, float p) {
    const float m = __builtin_amdgcn_exp2f(ar2 * p), t = __builtin_amdgcn_fractf(th * p);
    return f32x2{m * __builtin_amdgcn_cosf(t), m * __builtin_amdgcn_sinf(t)};
}

__global__ __launch_bounds__(768, 3) void fused_kernel(Params p) {
    extern __shared__ __attribute__((aligned(16))) unsigned char lds_raw[];
    const int tid = threadIdx.x, lane = tid & 63;
    const int v = __builtin_amdgcn_readfirstlane(tid >> 6);
    const int w = blockIdx.x, g = w & 7, j = w >> 3;
    const int pg = g & 1, gg = g >> 1;
    const int mp = 8 * pg + (j & 7), ng = gg + 4 * (j >> 3);
    const int U0 = ng < 11 ? 6 * ng : 66 + 4 * (ng - 11);
    const int nunits = ng < 11 ? 6 : 4;
    const int tb8 = 128 * pg + 32 * gg + j;
    const int d0 = 64 * g + 2 * j;
    const int rt = 4 * g + (j >> 3), ct = j & 7;
    unsigned char* ws = p.ws;
    const rsrc_t rs_ws = mk_rsrc(ws, WS_END);
    const int r32 = lane & 31, hh = lane >> 5;
    const int G = lane >> 4, q4 = (lane >> 2) & 3, p4 = lane & 3, gm = G & 1;
    STAMP(0); STAMPC(30);
    if (tid == 0) { lds_st<u32x2>(L_CNT, u32x2{0u, 0u}); }
    BARL();

    if (v >= 8) {
        const int hv = v - 8;
        const rsrc_t rs_x = mk_rsrc(p.x, L_ * D_ * 2);
        const int wm = hv >> 1, wr = hv & 1;
        const rsrc_t rs_w = mk_rsrc(wm ? p.wu : p.wg, D_ * F_ * 2);
        const unsigned vo_x = (lane >> 2) * 1024 + 16 * ((lane & 3) ^ ((lane >> 4) & 3));
        unsigned vo_w[3];
        #pragma unroll
        for (int i = 0; i < 3; ++i) { const int c = 64 * i + lane, row = c / 12, col = c - 12 * row; vo_w[i] = row * (F_ * 2) + col * 16; }
        auto stage1 = [&](int S) {
            const unsigned sb = (S % 7) * SLOT1;
            #pragma unroll
            for (int e = 0; e < 2; ++e) dma16(sb + (2 * hv + e) * 1024, vo_x, rs_x, (unsigned)((128 * mp + 32 * hv + 16 * e) * 1024 + 64 * S));
            #pragma unroll
            for (int i = 0; i < 3; ++i) dma16(sb + W_OFF + wm * W_M + wr * 3072 + i * 1024, vo_w[i], rs_w, (unsigned)((32 * S + 16 * wr) * (F_ * 2) + 32 * U0));
        };
        #pragma unroll
        for (int S = 0; S < 5; ++S) stage1(S);
        STAMP(1);
        WAITV(20); BAR();
        stage1(5);
        #pragma unroll
        for (int S = 0; S < 15; ++S) {
            if (S <= 10) WAITV(20); else if (S == 11) WAITV(15); else if (S == 12) WAITV(10); else if (S == 13) WAITV(5); else WAITV(0);
            BAR();
            if (S + 6 < 16) stage1(S + 6);
        }
        STAMP(8);
        const unsigned tag = lds_ld<unsigned>(L_SCL + 8);
        if (hv == 0) {
            #pragma unroll
            for (int i = 0; i < 4; ++i) wait_flags(ws, WS_FLAGX + 256 * i, 64, tag, lane);
            if (w == 0 && lane == 0) st_sc1_u32(ws + WS_EPOCH, tag);
        }
        grp_bar(L_CNT + 4, 4, lane);
        const int ht = tid - 512;
        #pragma unroll
        for (int ch = 0; ch < 2; ++ch) {
            const int tbk = ht;
            const u32x4 c = __builtin_amdgcn_raw_buffer_load_b128(rs_ws, WS_XNT + tbk * 8192 + (d0 + ch) * 16, 0, AUX_SC1);
            u32x4 n = {0u, 0u, 0u, 0u};
            if (tbk < 255) n = __builtin_amdgcn_raw_buffer_load_b128(rs_ws, WS_XNT + (tbk + 1) * 8192 + (d0 + ch) * 16, 0, AUX_SC1);
            auto copies = [&](const u32x4& c, const u32x4& n, int b) {
                const unsigned ub = L_U + ch * 4 * UCP + 16 * b;
                u32x4 o;
                o = u32x4{__builtin_amdgcn_alignbyte(c[1], c[0], 2), __builtin_amdgcn_alignbyte(c[2], c[1], 2), __builtin_amdgcn_alignbyte(c[3], c[2], 2), __builtin_amdgcn_alignbyte(n[0], c[3], 2)};
                lds_st<u32x4>(ub, o);
                o = u32x4{c[1], c[2], c[3], n[0]}; lds_st<u32x4>(ub + UCP, o);
                o = u32x4{__builtin_amdgcn_alignbyte(c[2], c[1], 2), __builtin_amdgcn_alignbyte(c[3], c[2], 2), __builtin_amdgcn_alignbyte(n[0], c[3], 2), __builtin_amdgcn_alignbyte(n[1], n[0], 2)};
                lds_st<u32x4>(ub + 2 * UCP, o);
                o = u32x4{c[2], c[3], n[0], n[1]}; lds_st<u32x4>(ub + 3 * UCP, o);
            };
            copies(c, n, tbk + 4);
            if (tbk < 4) {
                const u32x4 z = {0u, 0u, 0u, 0u};
                const u32x4 first = __builtin_amdgcn_raw_buffer_load_b128(rs_ws, WS_XNT + (d0 + ch) * 16, 0, AUX_SC1);
                copies(z, tbk == 3 ? first : z, tbk);
            }
        }
        #pragma unroll
        for (int it = 0; it < 3; ++it) {
            const int id = ht + 256 * it;
            if (id < 760) {
                const int c2 = id >= 380, qq = id - 380 * c2;
                u32x4 o = {0u, 0u, 0u, 0u};
                if (qq < 256) {
                    const int a = 255 - qq;
                    int base; unsigned sel;
                    if (a < 32) { base = 8 * a; sel = 0x76543210u; }
                    else if (a < 64) { base = 256 + 4 * (a - 32); sel = 0x43222100u; }
                    else if (a < 128) { base = 384 + 2 * (a - 64); sel = 0x22111000u; }
                    else { base = 512 + (a - 128); sel = (a & 1) ? 0x11110000u : 0x11100000u; }
                    const unsigned kp = L_KP + c2 * 1296 + base * 2;
                    #pragma unroll
                    for (int i = 0; i < 4; ++i) {
                        const unsigned lo16 = lds_ld<u16>(kp + 2 * ((sel >> (4 * (7 - 2 * i))) & 15));
                        const unsigned hi16 = lds_ld<u16>(kp + 2 * ((sel >> (4 * (6 - 2 * i))) & 15));
                        o[i] = lo16 | (hi16 << 16);
                    }
                }
                lds_st<u32x4>(L_KR + c2 * KRB + 16 * (qq ^ ((qq >> 4) & 3)), o);
            }
        }
        grp_bar(L_CNT + 4, 8, lane);
        STAMP(9);
        {
            const int ch = hv >> 1, half = hv & 1;
            f32x16 y0 = {}, y1 = {};
            const unsigned a_u = L_U + ch * 4 * UCP + q4 * UCP + 2 * (8 * hh + 16 * gm + 4 * p4) + 1024 * half;
            const int qb = 252 - 4 * r32 + 64 * half + hh;
            auto rdk = [&](int q) { return lds_ld<s16x8>(L_KR + ch * KRB + 16 * (q ^ ((q >> 4) & 3))); };
            s16x8 Ua = lds_tr2(a_u, a_u + 8), Ub = lds_tr2(a_u + 2048, a_u + 2056), K0 = rdk(qb), K1 = rdk(qb - 128);
            #pragma unroll 8
            for (int i = 0; i < 32; ++i) {
                const int in = i < 31 ? i + 1 : 31;
                y0 = MFMA_H(Ua, K0, y0);
                SB0();
                const s16x8 nUa = lds_tr2(a_u + 32 * in, a_u + 32 * in + 8);
                const s16x8 nK1 = rdk(qb + 2 * in - 128);
                SB0();
                y1 = MFMA_H(Ua, K1, y1);
                SB0();
                const s16x8 nUb = lds_tr2(a_u + 32 * in + 2048, a_u + 32 * in + 2056);
                SB0();
                y1 = MFMA_H(Ub, K0, y1);
                SB0();
                const s16x8 nK0 = rdk(qb + 2 * in);
                Ua = nUa; Ub = nUb; K0 = nK0; K1 = nK1;
                SB0();
            }
            STAMP(10);
            const unsigned rb = L_CRED + ch * CRED_W + r32 * 144 + hh * 16;
            #pragma unroll
            for (int gq = 0; gq < 4; ++gq) {
                if (half) lds_st<f32x4>(rb + gq * 32, f32x4{y0[4 * gq], y0[4 * gq + 1], y0[4 * gq + 2], y0[4 * gq + 3]});
                else lds_st<f32x4>(rb + 32 * 144 + gq * 32, f32x4{y1[4 * gq], y1[4 * gq + 1], y1[4 * gq + 2], y1[4 * gq + 3]});
            }
            grp_bar(L_CNT + 4, 12, lane);
            const float isc = lds_ld<float>(L_SCL + 4 * ch);
            const float dsk = bf2f(p.Dskip[d0 + ch]);
            const unsigned ob = rb + (half ? 32 * 144 : 0);
            #pragma unroll
            for (int gq = 0; gq < 4; ++gq) {
                const f32x4 o = lds_ld<f32x4>(ob + gq * 32);
                const int l0 = 32 * (r32 + 32 * half) + 8 * gq + 4 * hh;
                const unsigned ua = L_U + ch * 4 * UCP + 3 * UCP + 2 * (l0 + 28);
                const f16x2 u01 = lds_ld<f16x2>(ua), u23 = lds_ld<f16x2>(ua + 4);
                const float uu[4] = {(float)u01[0], (float)u01[1], (float)u23[0], (float)u23[1]};
                float r[4];
                #pragma unroll
                for (int i = 0; i < 4; ++i) { const float y = rbf(((half ? y1[4 * gq + i] : y0[4 * gq + i]) + o[i]) * isc); r[i] = y + rbf(uu[i] * dsk); }
                __builtin_amdgcn_raw_buffer_store_b64(u32x2{pk_bf(r[0], r[1]), pk_bf(r[2], r[3])}, rs_ws, WS_AT + (d0 + ch) * 4096 + l0 * 2, 0, AUX_SC1);
            }
            WAITV(0);
            grp_bar(L_CNT + 4, 16, lane);
            if (hv == 0 && lane == 0) st_sc1_u32(ws + WS_FLAGA + 4 * (d0 >> 1), tag);
        }
        STAMP(11);
        {
            const rsrc_t rs_wd = mk_rsrc(p.wd, D_ * F_ * 2);
            const unsigned vo_d = (lane >> 3) * 1024 + 16 * ((lane & 7) ^ (((lane >> 4) & 1) << 2));
            auto stage2 = [&](int st) {
                #pragma unroll
                for (int e = 0; e < 4; ++e) {
                    const int U = 8 * st + 2 * hv + (e >> 1);
                    dma16(G2S[st % 7] + (2 * hv + (e >> 1)) * 2048 + (e & 1) * 1024, vo_d, rs_wd, (unsigned)((16 * U + 8 * (e & 1)) * 1024 + 128 * ct));
                }
            };
            stage2(4); stage2(5); stage2(6);
            #pragma unroll
            for (int st = 0; st < 11; ++st) {
                if (st >= 3 && st <= 5) WAITV(16); else if (st == 6) WAITV(12); else if (st == 7) WAITV(8); else if (st == 8) WAITV(4); else if (st == 9) WAITV(0);
                BAR();
                if (st >= 1 && st + 6 < 11) stage2(st + 6);
            }
        }
        BARL(); BARL(); BARL();
        STAMP(16); STAMPC(31);
        STAMP_DUMP();
        return;
    }

    const int tb = v & 3, ug = v >> 2;
    unsigned tag;
    {
        const u32x4 xrow = *(const GAS u32x4*)((const unsigned char*)p.x + (size_t)(8 * tb8 + v) * 1024 + lane * 16);
        tag = __builtin_amdgcn_readfirstlane(ld_sc1_u32(ws + WS_EPOCH)) + 1u;
        const int ch = v & 1, nh = (v >> 1) & 1, part = v >> 2;
        const int d = d0 + ch, n = 64 * nh + lane;
        const unsigned pa = p.logA[d * NS_ + n], pi = p.Aim[d * NS_ + n], pcr = p.Cre[d * NS_ + n], pci = p.Cim[d * NS_ + n];
        const unsigned pcr2 = p.Cre[d * NS_ + (n ^ 64)], pci2 = p.Cim[d * NS_ + (n ^ 64)], pdt = p.logdt[d];
        if (tid == 0) lds_st<unsigned>(L_SCL + 8, tag);
        float xv[8];
        #pragma unroll
        for (int i = 0; i < 4; ++i) { xv[2 * i] = bf2f(xrow[i] & 0xFFFFu); xv[2 * i + 1] = __uint_as_float(xrow[i] & 0xFFFF0000u); }
        float s = 0.f;
        #pragma unroll
        for (int i = 0; i < 8; ++i) s += xv[i];
        const float mean = wave_sum(s) * (1.f / 512.f);
        float q = 0.f;
        #pragma unroll
        for (int i = 0; i < 8; ++i) { const float dd = xv[i] - mean; q = fmaf(dd, dd, q); }
        const float var = wave_sum(q) * (1.f / 512.f);
        const float mb = rbf(mean), sb = rbf(__builtin_sqrtf(rbf(rbf(var) + 1e-5f)));
        const float ri = 1.f / sb;
        u32x4 o;
        #pragma unroll
        for (int i = 0; i < 4; ++i) {
            float a = rbf(xv[2 * i] - mb), b = rbf(xv[2 * i + 1] - mb);
            float qa = a * ri, qb = b * ri;
            qa = fmaf(fmaf(-qa, sb, a), ri, qa); qb = fmaf(fmaf(-qb, sb, b), ri, qb);
            o[i] = pk_f16(rbf(qa), rbf(qb));
        }
        lds_st<u32x4>(L_LN + v * 1024 + lane * 16, o);
        if (lane == 0) {
            const unsigned long long gr = ((unsigned long long)tag << 32) | (f2bf(mb) | (f2bf(sb) << 16));
            __hip_atomic_store((GAS unsigned long long*)(ws + WS_STATS + (8 * tb8 + v) * 8), gr, RLX_AGENT);
        }
        STAMP(1);
        grp_bar(L_CNT, 8, lane);
        #pragma unroll
        for (int i = 0; i < 4; ++i) {
            const unsigned a = lds_ld<u16>(L_LN + (2 * i) * 1024 + tid * 2), b = lds_ld<u16>(L_LN + (2 * i + 1) * 1024 + tid * 2);
            o[i] = a | (b << 16);
        }
        __builtin_amdgcn_raw_buffer_store_b128(o, rs_ws, WS_XNT + tb8 * 8192 + tid * 16, 0, AUX_SC1);
        grp_bar(L_CNT, 16, lane);
        STAMP(2);

        const unsigned scr = L_KG + v * KG_W;
        const float dt = rbf(__expf(bf2f(pdt)));
        const float ar2 = -rbf(__expf(bf2f(pa))) * dt * 1.4426950408889634f;
        const float th = bf2f(pi) * dt * 0.15915494309189535f;
        const float cr = bf2f(pcr) * dt, ci = bf2f(pci) * dt;
        const float mx = wave_max(fmaxf(fmaxf(fabsf(cr), fabsf(ci)), fmaxf(fabsf(bf2f(pcr2) * dt), fabsf(bf2f(pci2) * dt))));
        int e = (int)((__float_as_uint(mx) >> 23) & 0xFF);
        e = e < 10 ? 10 : (e > 240 ? 240 : e);
        const float scale = __uint_as_float((unsigned)(259 - e) << 23);
        const float iscale = __uint_as_float((unsigned)(e - 5) << 23);
        if (nh == 0 && part == 0 && lane == 0) lds_st<float>(L_SCL + 4 * ch, iscale);
        const f32x2 c0 = {cr * (2.f * scale), ci * (2.f * scale)};
        const f32x2 z1 = cexp_p(ar2, th, 1.f), z16 = cexp_p(ar2, th, 16.f);
        auto put = [&](int row, f32x2 val, bool neg) { lds_st<unsigned>(scr + row * 272 + lane * 4, pk_f16(val.x, neg ? -val.y : val.y)); };
        const int fr = lane & 15, fq = lane >> 4;
        auto frag = [&](int ks) { return lds_ld<f16x8>(scr + fr * 272 + ks * 64 + fq * 16); };
        { f32x2 t = {1.f, 0.f}; put(0, t, false); t = z16; put(1, t, false);
          #pragma unroll
          for (int c = 2; c < 16; ++c) { t = cmul(t, z16); put(c, t, false); } }
        f16x8 lo[4];
        #pragma unroll
        for (int ks = 0; ks < 4; ++ks) lo[ks] = frag(ks);
        WAITL();
        #pragma unroll
        for (int ks = 0; ks < 4; ++ks) asm volatile("" : "+v"(lo[ks]));
        auto tile = [&]() {
            f32x4 a = {0.f, 0.f, 0.f, 0.f};
            #pragma unroll
            for (int ks = 0; ks < 4; ++ks) a = __builtin_amdgcn_mfma_f32_16x16x32_f16(frag(ks), lo[ks], a, 0, 0, 0);
            asm volatile("" : "+v"(a));
            WAITL();
            return a;
        };
        f32x4 ka, kb = {0.f, 0.f, 0.f, 0.f};
        if (part == 0) {
            f32x2 t = c0; put(0, t, true);
            #pragma unroll
            for (int i = 1; i < 16; ++i) { t = cmul(t, z1); put(i, t, true); }
            ka = tile();
        } else {
            const f32x2 z256 = cexp_p(ar2, th, 256.f);
            const f32x2 z2 = cmul(z1, z1), z4 = cmul(z2, z2), z8 = cmul(z4, z4);
            f32x2 b = cmul(c0, z256), t = b; put(0, t, true);
            #pragma unroll
            for (int i = 1; i < 8; ++i) { t = cmul(t, z2); put(i, t, true); }
            #pragma unroll
            for (int a = 0; a < 2; ++a) { b = cmul(b, z256); t = b; put(8 + 4 * a, t, true);
                #pragma unroll
                for (int i = 1; i < 4; ++i) { t = cmul(t, z4); put(8 + 4 * a + i, t, true); } }
            ka = tile();
            #pragma unroll
            for (int a = 0; a < 4; ++a) { b = cmul(b, z256); put(2 * a, b, true); put(2 * a + 1, cmul(b, z8), true); }
            b = cmul(b, z256); put(8, b, true);
            #pragma unroll
            for (int i = 9; i < 16; ++i) put(i, f32x2{0.f, 0.f}, false);
            kb = tile();
        }
        if (nh == 1) { lds_st<f32x4>(scr + lane * 16, ka); lds_st<f32x4>(scr + 1024 + lane * 16, kb); }
        grp_bar(L_CNT, 24, lane);
        if (nh == 0) {
            const unsigned oscr = scr + 2 * KG_W;
            #pragma unroll
            for (int tt = 0; tt < 2; ++tt) {
                if (tt == 0 || part == 1) {
                    const f32x4 ov = lds_ld<f32x4>(oscr + tt * 1024 + lane * 16);
                    #pragma unroll
                    for (int r = 0; r < 4; ++r) {
                        const int i = 4 * fq + r, c = fr;
                        int idx;
                        if (part == 0) idx = 16 * c + i;
                        else if (tt == 0) idx = i < 8 ? 256 + 8 * c + i : (i < 12 ? 384 + 4 * c + (i - 8) : 448 + 4 * c + (i - 12));
                        else idx = i < 8 ? 512 + 32 * (i >> 1) + 2 * c + (i & 1) : ((i == 8 && c == 0) ? 640 : 644);
                        lds_st<_Float16>(L_KP + ch * 1296 + idx * 2, (_Float16)((tt ? kb[r] : ka[r]) + ov[r]));
                    }
                }
            }
        }
    }
    STAMP(3);
    BARL();
    STAMP(4);

    f32x16 acc[3] = {};
    float cs[3] = {0.f, 0.f, 0.f};
    unsigned long long gran = 0;
    {
        const unsigned xs = (r32 >> 2) & 3;
        const unsigned ax = (32 * tb + r32) * 64;
        unsigned bx[3][2], bw[3];
        #pragma unroll
        for (int b = 0; b < 3; ++b) {
            bx[b][0] = ax + 16 * ((0 + hh) ^ xs) + b * 3 * SLOT1; bx[b][1] = ax + 16 * ((2 + hh) ^ xs) + b * 3 * SLOT1;
            bw[b] = W_OFF + gm * W_M + (8 * hh + q4) * 192 + 96 * ug + 16 * (p4 >> 1) + 8 * (p4 & 1) + b * 3 * SLOT1;
            asm volatile("" : "+v"(bx[b][0]), "+v"(bx[b][1]), "+v"(bw[b]));
        }
        auto rdx = [&](int kg) { const int s = (kg >> 1) % 7; return lds_ld<s16x8>(bx[s / 3][kg & 1] + (s % 3) * SLOT1); };
        auto rdw = [&](int kg, int uu) { const int s = (kg >> 1) % 7; const unsigned a = bw[s / 3] + (s % 3) * SLOT1 + (kg & 1) * 3072 + 32 * uu; return lds_tr2(a, a + 768); };
        const bf16x2 ones = {(__bf16)1.0f, (__bf16)1.0f};
        auto csum = [&](float& c, const s16x8& a) {
            const u32x4 au = __builtin_bit_cast(u32x4, a);
            #pragma unroll
            for (int i = 0; i < 4; ++i) { const unsigned t = au[i]; c = __builtin_amdgcn_fdot2_f32_bf16(__builtin_bit_cast(bf16x2, t), ones, c, false); }
            asm volatile("" : "+v"(c));
        };
        s16x8 A[3][3], B[3];
        #pragma unroll
        for (int uu = 0; uu < 3; ++uu) A[0][uu] = rdw(0, uu);
        B[0] = rdx(0);
        #pragma unroll
        for (int S = 0; S < 16; ++S) {
            if (S == 2) WAITV(0);
            if (S < 15) BAR();
            if (S == 2 && tid == 0) st_sc1_u32(ws + WS_FLAGX + 4 * tb8, tag);
            if (S == 0) {
                #pragma unroll
                for (int uu = 0; uu < 3; ++uu) A[1][uu] = rdw(1, uu);
                B[1] = rdx(1);
            }
            if (S == 12) asm volatile("global_load_dwordx2 %0, %1, off sc1" : "=v"(gran) : "v"(ws + WS_STATS + (128 * mp + 32 * tb + r32) * 8) : "memory");
            #pragma unroll
            for (int ks = 0; ks < 2; ++ks) {
                const int kg = 2 * S + ks, c = kg % 3, nx = (kg + 2) % 3;
                acc[0] = MFMA_BF(A[c][0], B[c], acc[0]);
                SB0();
                if (kg + 2 < 32) { A[nx][0] = rdw(kg + 2, 0);
 B[nx] = rdx(kg + 2);
 }
                SB0();
                acc[1] = MFMA_BF(A[c][1], B[c], acc[1]);
                SB0();

 if (kg + 2 < 32) A[nx][1] = rdw(kg + 2, 1);

                SB0();
                acc[2] = MFMA_BF(A[c][2], B[c], acc[2]);
                SB0();

 if (kg + 2 < 32) A[nx][2] = rdw(kg + 2, 2);

                if ((kg & 3) == tb) { csum(cs[0], A[c][0]); csum(cs[1], A[c][1]); csum(cs[2], A[c][2]); }
                SB0();
            }
        }
    }
    STAMP(5);
    #pragma unroll
    for (int uu = 0; uu < 3; ++uu) lds_st<float>(L_PART + ((3 * ug + uu) * 32 + r32) * 32 + (2 * tb + hh) * 4, cs[uu]);
    WAITV(0);
    asm volatile("" : "+v"(gran));
    for (unsigned spins = 0; !__all((unsigned)(gran >> 32) == tag);) {
        if (spin_fail(spins, ws, tag)) break;
        gran = __hip_atomic_load((const GAS unsigned long long*)(ws + WS_STATS + (128 * mp + 32 * tb + r32) * 8), RLX_AGENT);
    }
    const float t_mean = bf2f((unsigned)gran & 0xFFFFu), t_ri = 1.f / __uint_as_float((unsigned)gran & 0xFFFF0000u);
    grp_bar(L_CNT, 32, lane);
    {
        const unsigned nm = pk_bf(-t_mean, -t_mean);
        const s16x8 Bm = __builtin_bit_cast(s16x8, u32x4{nm, nm, nm, nm});
        #pragma unroll
        for (int uu = 0; uu < 3; ++uu) {
            const unsigned pa = L_PART + ((3 * ug + uu) * 32 + r32) * 32;
            const f32x4 p0 = lds_ld<f32x4>(pa), p1 = lds_ld<f32x4>(pa + 16);
            u32x4 aw;
            #pragma unroll
            for (int i = 0; i < 4; ++i) {
                float a = i < 2 ? p0[2 * i] : p1[2 * i - 4], b = i < 2 ? p0[2 * i + 1] : p1[2 * i - 3];
                const unsigned hi = pk_bf(a, b);
                const unsigned lo = pk_bf(a - bf2f(hi & 0xFFFFu), b - __uint_as_float(hi & 0xFFFF0000u));
                aw[i] = hh ? lo : hi;
            }
            acc[uu] = MFMA_BF(__builtin_bit_cast(s16x8, aw), Bm, acc[uu]);
        }
        #pragma unroll
        for (int uu = 0; uu < 3; ++uu) {
            float hv[8];
            #pragma unroll
            for (int i = 0; i < 8; ++i) {
                const float gv = acc[uu][i] * t_ri, uv = acc[uu][8 + i] * t_ri;
                const float zz = gv * fmaf(gv * gv, -0.10294324f, -2.3022082f);
                hv[i] = gv * __builtin_amdgcn_rcpf(1.f + __builtin_amdgcn_exp2f(zz)) * uv;
            }
            const u32x4 o = {pk_bf(hv[0], hv[1]), pk_bf(hv[2], hv[3]), pk_bf(hv[4], hv[5]), pk_bf(hv[6], hv[7])};
            if (3 * ug + uu < nunits) __builtin_amdgcn_raw_buffer_store_b128(o, rs_ws, WS_H + ((4 * mp + tb) * 86 + U0 + 3 * ug + uu) * 1024 + lane * 16, 0, AUX_SC1);
        }
    }
    STAMP(6);
    WAITV(0);
    grp_bar(L_CNT, 40, lane);
    if (tid == 0) st_sc1_u32(ws + WS_FLAGH + mp * 64 + 4 * ng, tag);
    STAMP(7);

    {
        const int th = v & 1, kq = v >> 1;
        const rsrc_t rs_wd = mk_rsrc(p.wd, D_ * F_ * 2);
        const unsigned vo_d = (lane >> 3) * 1024 + 16 * ((lane & 7) ^ (((lane >> 4) & 1) << 2));
        #pragma unroll
        for (int st = 0; st < 4; ++st) {
            const int U = 8 * st + v;
            dma16(G2S[st] + v * 2048, vo_d, rs_wd, (unsigned)(16 * U * 1024 + 128 * ct));
            dma16(G2S[st] + v * 2048 + 1024, vo_d, rs_wd, (unsigned)((16 * U + 8) * 1024 + 128 * ct));
        }
        wait_flags(ws, WS_FLAGH + (rt >> 1) * 64, 16, tag, lane);
        STAMP(12);
        const unsigned hbase = WS_H + (2 * rt + th) * 86 * 1024 + kq * 1024;
        u32x4 hf[22];
        const unsigned vl16 = lane * 16;
        auto ldh = [&](int i) {
            const int U = 8 * (i >> 1) + 4 * (i & 1);
            const unsigned so = (U + 3 < 86 || kq < 2) ? hbase + U * 1024 : hbase;
            asm volatile("buffer_load_dwordx4 %0, %1, %2, %3 offen sc1" : "=v"(hf[i]) : "v"(vl16), "s"(rs_ws), "s"(so) : "memory");
        };
        constexpr int HD = 8;
        #pragma unroll
        for (int i = 0; i < HD; ++i) ldh(i);
        f32x16 d0a = {}, d1a = {};
        const unsigned sw = ((q4 >> 1) & 1) << 2;
        unsigned bd[2][2];
        #pragma unroll
        for (int b = 0; b < 2; ++b)
            #pragma unroll
            for (int nb = 0; nb < 2; ++nb) { bd[b][nb] = kq * 2048 + (4 * hh + q4) * 128 + 16 * ((4 * nb + 2 * gm + (p4 >> 1)) ^ sw) + 8 * (p4 & 1) + b * 65536; asm volatile("" : "+v"(bd[b][nb])); }
        auto rdd = [&](int i, int nb) { const unsigned s = G2S[(i >> 1) % 7] + (i & 1) * 8192; const unsigned a = bd[s >> 16][nb] + (s & 0xFFFFu); return lds_tr2(a, a + 1024); };
        WAITV(HD);
        grp_bar(L_CNT, 48, lane);
        STAMP(13);
        s16x8 W0 = rdd(0, 0), W1 = rdd(0, 1);
        #pragma unroll
        for (int st = 0; st < 11; ++st) {
            BAR();
            #pragma unroll
            for (int e = 0; e < 2; ++e) {
                const int i = 2 * st + e;
                if (i + HD < 22) ldh(i + HD);
                const int rem = (i + HD < 22) ? HD : 21 - i;
                asm volatile("s_waitcnt vmcnt(%1)" : "+v"(hf[i]) : "i"(rem) : "memory");
                const s16x8 H = __builtin_bit_cast(s16x8, hf[i]);
                s16x8 N0 = W0, N1 = W1;
                if (i < 21 || kq < 2) {
                    d0a = MFMA_BF(W0, H, d0a);
                    SB0();
                    if (i + 1 < 22) N0 = rdd(i + 1, 0);
                    SB0();
                    d1a = MFMA_BF(W1, H, d1a);
                    SB0();
                    if (i + 1 < 22) N1 = rdd(i + 1, 1);
                }
                W0 = N0; W1 = N1;
                SB0();
            }
        }
        STAMP(14);
        BARL();
        const unsigned rb = L_RED + kq * RED_W + (32 * th + r32) * 272 + hh * 16;
        #pragma unroll
        for (int gq = 0; gq < 4; ++gq) {
            lds_st<f32x4>(rb + gq * 32, f32x4{d0a[4 * gq], d0a[4 * gq + 1], d0a[4 * gq + 2], d0a[4 * gq + 3]});
            lds_st<f32x4>(rb + 128 + gq * 32, f32x4{d1a[4 * gq], d1a[4 * gq + 1], d1a[4 * gq + 2], d1a[4 * gq + 3]});
        }
        wait_flags(ws, WS_FLAGA + 4 * 32 * ct, 32, tag, lane);
        const u32x4 at = __builtin_amdgcn_raw_buffer_load_b128(rs_ws, WS_AT + (64 * ct + (tid >> 3)) * 4096 + (64 * rt + 8 * (tid & 7)) * 2, 0, AUX_SC1);
        const int tk = tid >> 3, ck = tid & 7;
        const u32x4 xv = *(const GAS u32x4*)((const unsigned char*)p.x + (size_t)(64 * rt + tk) * 1024 + (64 * ct + 8 * ck) * 2);
        #pragma unroll
        for (int i = 0; i < 4; ++i) lds_st<unsigned>(L_ATT + (tid >> 3) * 132 + (tid & 7) * 16 + 4 * i, at[i]);
        BARL();
        STAMP(15);
        f32x4 s0 = {0.f, 0.f, 0.f, 0.f}, s1 = s0;
        #pragma unroll
        for (int q = 0; q < 4; ++q) {
            const unsigned a = L_RED + q * RED_W + tk * 272 + ck * 32;
            s0 += lds_ld<f32x4>(a); s1 += lds_ld<f32x4>(a + 16);
        }
        float o[8];
        #pragma unroll
        for (int i = 0; i < 8; ++i) {
            const float av = bf2f(lds_ld<u16>(L_ATT + (8 * ck + i) * 132 + tk * 2));
            const float xx = (i & 1) ? __uint_as_float(xv[i >> 1] & 0xFFFF0000u) : bf2f(xv[i >> 1] & 0xFFFFu);
            o[i] = rbf(xx + av) + rbf(i < 4 ? s0[i & 3] : s1[i & 3]);
        }
        const u32x4 ov = {pk_bf(o[0], o[1]), pk_bf(o[2], o[3]), pk_bf(o[4], o[5]), pk_bf(o[6], o[7])};
        *(GAS u32x4*)((unsigned char*)p.out + (size_t)(64 * rt + tk) * 1024 + (64 * ct + 8 * ck) * 2) = ov;
        BARL();
    }
    STAMP(16); STAMPC(31);
    STAMP_DUMP();
}

extern "C" void kernel_launch(void* const* d_in, const int* in_sizes, int n_in, void* d_out, int out_size, void* d_ws, size_t ws_size, hipStream_t stream) {
    (void)in_sizes; (void)n_in; (void)out_size; (void)ws_size;
    Params p;
    p.x = (const u16*)d_in[0]; p.logA = (const u16*)d_in[1]; p.Aim = (const u16*)d_in[2]; p.Cre = (const u16*)d_in[3]; p.Cim = (const u16*)d_in[4];
    p.logdt = (const u16*)d_in[5]; p.Dskip = (const u16*)d_in[6]; p.wg = (const u16*)d_in[8]; p.wu = (const u16*)d_in[9]; p.wd = (const u16*)d_in[10];
    p.out = (u16*)d_out; p.ws = (unsigned char*)d_ws;
    (void)hipFuncSetAttribute((const void*)fused_kernel, hipFuncAttributeMaxDynamicSharedMemorySize, LDS_BYTES);
    fused_kernel<<<256, 768, LDS_BYTES, stream>>>(p);
}
```

```cpp
#define AUXO 17
#include <hip/hip_runtime.h>
#include <stdint.h>

#define LAS __attribute__((address_space(3)))
#define GAS __attribute__((address_space(1)))
#define DI __device__ __forceinline__
typedef unsigned short u16;
typedef short s16x4 __attribute__((ext_vector_type(4)));
typedef short s16x8 __attribute__((ext_vector_type(8)));
typedef _Float16 f16x8 __attribute__((ext_vector_type(8)));
typedef _Float16 f16x2 __attribute__((ext_vector_type(2)));
typedef __bf16 bf16x2 __attribute__((ext_vector_type(2)));
typedef float f32x2 __attribute__((ext_vector_type(2)));
typedef float f32x4 __attribute__((ext_vector_type(4)));
typedef float f32x16 __attribute__((ext_vector_type(16)));
typedef unsigned u32x2 __attribute__((ext_vector_type(2)));
typedef unsigned u32x4 __attribute__((ext_vector_type(4)));
typedef __amdgpu_buffer_rsrc_t rsrc_t;

constexpr int L_ = 2048, D_ = 512, NS_ = 128, F_ = 1376;
constexpr unsigned WS_EPOCH = 0, WS_TMO = 256, WS_FLAGX = 1024, WS_FLAGH = 4096, WS_FLAGA = 8192, WS_STATS = 16384;
constexpr unsigned WS_XNT = 65536, WS_H = WS_XNT + 2097152, WS_AT = WS_H + 64 * 86 * 1024, WS_END = WS_AT + 2097152;
constexpr unsigned WS_STAMP = 16u << 20;
constexpr unsigned XSLOT = 16384, W_RING = 65536, WSLOT = 12352, W_M = 6176, MISC = W_RING + 6 * WSLOT;
constexpr unsigned L_KP = MISC;
constexpr unsigned L_SCL = L_KP + 2592;
constexpr unsigned L_CNT = L_SCL + 16;
constexpr unsigned L_PART = L_CNT + 64;
constexpr unsigned L_LN = L_PART;
constexpr unsigned L_KG = L_LN + 8192;
constexpr unsigned L_STAMP = L_KG + 8192;
constexpr unsigned L_END = L_STAMP + 768;
static_assert(L_END <= 163840, "lds");
constexpr unsigned UCP = 4160, L_U = 0;
constexpr unsigned KRB = 6080, L_KR = L_U + 2 * 4 * UCP;
constexpr unsigned CRED_W = 64 * 144, L_CRED = W_RING + 4 * WSLOT;
static_assert(L_KR + 2 * KRB <= 3 * XSLOT && L_CRED + 2 * CRED_W <= MISC, "conv lds");
constexpr unsigned G2S[7] = {45440, 61824, 78208, 94592, 0, 16384, L_CRED};
static_assert(L_KR + 2 * KRB <= 45440 && 94592 + 16384 <= L_CRED && L_CRED + 16384 <= MISC, "g2 slots");
constexpr unsigned L_RED = 0, RED_W = 64 * 272, L_ATT = 4 * RED_W;
static_assert(L_ATT + 64 * 132 <= MISC, "g2 lds");
constexpr int LDS_BYTES = 163840;
constexpr unsigned SPIN_CAP = 1u << 16;
constexpr int AUX_SC1 = 16;

struct Params {
    const u16* x; const u16* logA; const u16* Aim; const u16* Cre; const u16* Cim; const u16* logdt; const u16* Dskip;
    const u16* wg; const u16* wu; const u16* wd; u16* out; unsigned char* ws;
};

DI float bf2f(unsigned b) { return __uint_as_float(b << 16); }
DI unsigned pk_bf(float a, float b) { f32x2 v = {a, b}; return __builtin_bit_cast(unsigned, __builtin_convertvector(v, bf16x2)); }
DI unsigned f2bf(float f) { return pk_bf(f, 0.f) & 0xFFFFu; }
DI unsigned pk_f16(float a, float b) { f32x2 v = {a, b}; return __builtin_bit_cast(unsigned, __builtin_convertvector(v, f16x2)); }
DI float rbf(float f) { return bf2f(f2bf(f)); }
#define DPPF(v, ctrl) __builtin_bit_cast(float, __builtin_amdgcn_update_dpp(0, __builtin_bit_cast(int, (v)), (ctrl), 0xF, 0xF, true))
DI float rdl(float v, int l) { return __builtin_bit_cast(float, __builtin_amdgcn_readlane(__builtin_bit_cast(int, v), l)); }
DI float wave_sum(float v) {
    v += DPPF(v, 0xB1); v += DPPF(v, 0x4E); v += DPPF(v, 0x141); v += DPPF(v, 0x140);
    return (rdl(v, 0) + rdl(v, 16)) + (rdl(v, 32) + rdl(v, 48));
}
DI float wave_max(float v) {
    v = fmaxf(v, DPPF(v, 0xB1)); v = fmaxf(v, DPPF(v, 0x4E)); v = fmaxf(v, DPPF(v, 0x141)); v = fmaxf(v, DPPF(v, 0x140));
    return fmaxf(fmaxf(rdl(v, 0), rdl(v, 16)), fmaxf(rdl(v, 32), rdl(v, 48)));
}
DI rsrc_t mk_rsrc(const void* p, unsigned bytes) { return __builtin_amdgcn_make_buffer_rsrc((void*)p, 0, bytes, 0x00020000); }
DI void dma16(unsigned lds, unsigned voff, rsrc_t rs, unsigned soff) {
    asm volatile("s_mov_b32 m0, %0\n\ts_nop 0\n\tbuffer_load_dwordx4 %1, %2, %3 offen lds" :: "s"(lds), "v"(voff), "s"(rs), "s"(soff) : "memory");
}
#define WAITV(n) asm volatile("s_waitcnt vmcnt(%0)" :: "i"(n) : "memory")
#define WAITL() asm volatile("s_waitcnt lgkmcnt(0)" ::: "memory")
#define SB0() __builtin_amdgcn_sched_barrier(0)
#define BAR() do { SB0(); asm volatile("s_barrier" ::: "memory"); SB0(); } while (0)
#define BARL() do { SB0(); asm volatile("s_waitcnt lgkmcnt(0)\n\ts_barrier" ::: "memory"); SB0(); } while (0)
template <class T> DI T lds_ld(unsigned a) { return *(const LAS T*)(size_t)a; }
template <class T> DI void lds_st(unsigned a, T v) { *(LAS T*)(size_t)a = v; }
DI s16x8 lds_tr2(unsigned a0, unsigned a1) {
    s16x4 lo = __builtin_amdgcn_ds_read_tr16_b64_v4i16((LAS s16x4*)(size_t)a0);
    s16x4 hi = __builtin_amdgcn_ds_read_tr16_b64_v4i16((LAS s16x4*)(size_t)a1);
    return __builtin_shufflevector(lo, hi, 0, 1, 2, 3, 4, 5, 6, 7);
}
DI void grp_bar(unsigned cnt, unsigned target, int lane) {
    SB0();
    asm volatile("s_waitcnt lgkmcnt(0)" ::: "memory");
    if (lane == 0) { const unsigned one = 1; asm volatile("ds_add_u32 %0, %1" :: "v"(cnt), "v"(one) : "memory"); }
    for (unsigned it = 0; it < (1u << 22); ++it) {
        unsigned c;
        asm volatile("ds_read_b32 %0, %1\n\ts_waitcnt lgkmcnt(0)" : "=v"(c) : "v"(cnt) : "memory");
        if (__builtin_amdgcn_readfirstlane(c) >= target) break;
    }
    SB0();
}
#define MFMA_BF(a, b, c) __builtin_amdgcn_mfma_f32_32x32x16_bf16((a), (b), (c), 0, 0, 0)
#define MFMA_H(a, b, c) __builtin_amdgcn_mfma_f32_32x32x16_f16(__builtin_bit_cast(f16x8, a), __builtin_bit_cast(f16x8, b), (c), 0, 0, 0)
#define RLX_AGENT __ATOMIC_RELAXED, __HIP_MEMORY_SCOPE_AGENT
DI unsigned ld_sc1_u32(const unsigned char* p) { return __hip_atomic_load((const GAS unsigned*)p, RLX_AGENT); }
DI void st_sc1_u32(unsigned char* p, unsigned v) { __hip_atomic_store((GAS unsigned*)p, v, RLX_AGENT); }
DI bool spin_fail(unsigned& spins, unsigned char* ws, unsigned tag) {
    __builtin_amdgcn_s_sleep(1);
    if (++spins > SPIN_CAP) { st_sc1_u32(ws + WS_TMO, tag); return true; }
    return (spins & 255u) == 0u && __builtin_amdgcn_readfirstlane(ld_sc1_u32(ws + WS_TMO)) == tag;
}
DI void wait_flags(unsigned char* ws, unsigned off, int nw, unsigned tag, int lane) {
    for (unsigned spins = 0;;) {
        unsigned v = tag;
        if (lane < nw) v = ld_sc1_u32(ws + off + 4 * lane);
        if (__all(v == tag)) return;
        if (spin_fail(spins, ws, tag)) return;
    }
}
#define STAMP(i) do { } while (0)
#define STAMPC(i) do { } while (0)
#define STAMP_DUMP() do { } while (0)

DI f32x2 cmul(f32x2 a, f32x2 b) { const f32x2 t = a.xx * b; const f32x2 bs = {-b.y, b.x}; return a.yy * bs + t; }
DI f32x2 cexp_p(float ar2, float th, float p) {
    const float m = __builtin_amdgcn_exp2f(ar2 * p), t = __builtin_amdgcn_fractf(th * p);
    return f32x2{m * __builtin_amdgcn_cosf(t), m * __builtin_amdgcn_sinf(t)};
}

template <int NU>
DI void gemm1_loop(f32x16 (&acc)[3], float (&cs)[3], unsigned long long& gran, int tb, int ug, int lane, int tid, unsigned tag, unsigned char* ws, unsigned flagx_off, unsigned stat_off) {
    const int r32 = lane & 31, hh = lane >> 5, G = lane >> 4, q4 = (lane >> 2) & 3, p4 = lane & 3, gm = G & 1;
    unsigned bx[4], bw[2];
    #pragma unroll
    for (int ks = 0; ks < 4; ++ks) { bx[ks] = (32 * tb + r32) * 128 + 16 * (((2 * ks) | hh) ^ ((r32 >> 1) & 7)); asm volatile("" : "+v"(bx[ks])); }
    #pragma unroll
    for (int b = 0; b < 2; ++b) { bw[b] = W_RING + b * 3 * WSLOT + gm * W_M + (8 * hh + q4) * 192 + 96 * ug + 16 * (p4 >> 1) + 8 * (p4 & 1); asm volatile("" : "+v"(bw[b])); }
    auto rdx = [&](int kg) { return lds_ld<s16x8>(bx[kg & 3] + ((kg >> 2) & 3) * XSLOT); };
    auto rdw = [&](int kg, int uu) { const int s = (kg >> 1) % 6; const unsigned a = bw[s / 3] + (s % 3) * WSLOT + (kg & 1) * 3072 + 32 * uu; return lds_tr2(a, a + 768); };
    const bf16x2 ones = {(__bf16)1.0f, (__bf16)1.0f};
    auto csum = [&](float& c, const s16x8& a) {
        const u32x4 au = __builtin_bit_cast(u32x4, a);
        #pragma unroll
        for (int i = 0; i < 4; ++i) { const unsigned t = au[i]; c = __builtin_amdgcn_fdot2_f32_bf16(__builtin_bit_cast(bf16x2, t), ones, c, false); }
        asm volatile("" : "+v"(c));
    };
    s16x8 A[3][NU], B[3];
    #pragma unroll
    for (int k0 = 0; k0 < 2; ++k0) {
        #pragma unroll
        for (int uu = 0; uu < NU; ++uu) A[k0][uu] = rdw(k0, uu);
        B[k0] = rdx(k0);
    }
    #pragma unroll
    for (int S = 0; S < 16; ++S) {
        if (S == 2) WAITV(0);
        if (S < 14) BAR();
        if (S == 2 && tid == 0) st_sc1_u32(ws + flagx_off, tag);
        if (S == 12) asm volatile("global_load_dwordx2 %0, %1, off sc1" : "=v"(gran) : "v"(ws + stat_off) : "memory");
        #pragma unroll
        for (int ks = 0; ks < 2; ++ks) {
            const int kg = 2 * S + ks, c = kg % 3, nx = (kg + 2) % 3;
            acc[0] = MFMA_BF(A[c][0], B[c], acc[0]);
            SB0();
            if (kg + 2 < 32) { A[nx][0] = rdw(kg + 2, 0); B[nx] = rdx(kg + 2); }
            SB0();
            if constexpr (NU == 3) {
                acc[1] = MFMA_BF(A[c][1], B[c], acc[1]);
                SB0();
                if (kg + 2 < 32) A[nx][1] = rdw(kg + 2, 1);
                SB0();
                acc[2] = MFMA_BF(A[c][2], B[c], acc[2]);
                SB0();
                if (kg + 2 < 32) A[nx][2] = rdw(kg + 2, 2);
            }
            if ((kg & 3) == tb) {
                #pragma unroll
                for (int uu = 0; uu < NU; ++uu) csum(cs[uu], A[c][uu]);
            }
            SB0();
        }
    }
}

struct G1Sched { int nw[14]; };
constexpr G1Sched g1_sched() {
    G1Sched r{};
    int wend[16] = {14, 20, 34, 40, 54, 60}, xend[8] = {8, 28, 48, 68}, cum = 68;
    int wi[16] = {}, xi[8] = {};
    {
        int c = 68;
        for (int S = 0; S < 14; ++S) {
            if (S >= 1 && S <= 10) { c += 6; wend[S + 5] = c; }
            if (S >= 2 && S <= 9) { c += 4; xend[(S + 6) >> 1] = c; }
        }
    }
    (void)wi; (void)xi;
    for (int S = 0; S < 14; ++S) {
        int need = wend[S + 1] > xend[(S + 1) >> 1] ? wend[S + 1] : xend[(S + 1) >> 1];
        if (S == 13) need = cum;
        r.nw[S] = cum - need;
        if (S >= 1 && S <= 10) cum += 6;
        if (S >= 2 && S <= 9) cum += 4;
    }
    return r;
}
template <int DUMMY>
DI void gemm1_loader(int a, int lane, const Params& p, int mp, int U0) {
    const rsrc_t rs_x = mk_rsrc(p.x, L_ * D_ * 2);
    const rsrc_t rs_w = mk_rsrc(a ? p.wu : p.wg, D_ * F_ * 2);
    unsigned vo_x[2], vo_w[3];
    #pragma unroll
    for (int e = 0; e < 2; ++e) vo_x[e] = (lane >> 3) * 1024 + 16 * ((lane & 7) ^ ((4 * e + (lane >> 4)) & 7));
    #pragma unroll
    for (int i = 0; i < 3; ++i) { const int c = 64 * i + lane, row = c / 12, col = c - 12 * row; vo_w[i] = row * (F_ * 2) + col * 16; }
    auto xhalf = [&](int xa, int hf) {
        #pragma unroll
        for (int i = 0; i < 4; ++i)
            dma16((xa & 3) * XSLOT + (8 * hf + 4 * a + i) * 1024, vo_x[i & 1], rs_x, (unsigned)((128 * mp + 64 * hf + 32 * a + 8 * i) * 1024 + 128 * xa));
    };
    auto wst = [&](int S) {
        #pragma unroll
        for (int i = 0; i < 6; ++i)
            dma16(W_RING + (S % 6) * WSLOT + a * W_M + i * 1024, vo_w[i % 3], rs_w, (unsigned)((32 * S + 16 * (i / 3)) * (F_ * 2) + 32 * U0));
    };
    xhalf(0, 0); xhalf(0, 1); wst(0); wst(1); xhalf(1, 0); xhalf(1, 1); wst(2); wst(3); xhalf(2, 0); xhalf(2, 1); wst(4); wst(5); xhalf(3, 0); xhalf(3, 1);
    WAITV(54); BAR();
    constexpr G1Sched sc = g1_sched();
    #pragma unroll
    for (int S = 0; S < 14; ++S) {
        WAITV(sc.nw[S]);
        BAR();
        if (S >= 1 && S <= 10) wst(S + 5);
        if (S >= 2 && S <= 9) xhalf((S + 6) >> 1, (S + 6) & 1);
    }
}

__global__ __launch_bounds__(768, 3) void fused_kernel(Params p) {
    extern __shared__ __attribute__((aligned(16))) unsigned char lds_raw[];
    const int tid = threadIdx.x, lane = tid & 63;
    const int v = __builtin_amdgcn_readfirstlane(tid >> 6);
    const int w = blockIdx.x, g = w & 7, j = w >> 3;
    const int pg = g & 1, gg = g >> 1;
    const int mp = 8 * pg + (j & 7), ng = gg + 4 * (j >> 3);
    const int U0 = ng < 11 ? 6 * ng : 66 + 4 * (ng - 11);
    const int nunits = ng < 11 ? 6 : 4;
    const int tb8 = 128 * pg + 32 * gg + j;
    const int d0 = 64 * g + 2 * j;
    const int rt = 4 * g + (j >> 3), ct = j & 7;
    unsigned char* ws = p.ws;
    const rsrc_t rs_ws = mk_rsrc(ws, WS_END);
    const int r32 = lane & 31, hh = lane >> 5;
    const int G = lane >> 4, q4 = (lane >> 2) & 3, p4 = lane & 3, gm = G & 1;
    STAMP(0); STAMPC(30);
    if (tid == 0) { lds_st<u32x2>(L_CNT, u32x2{0u, 0u}); }
    BARL();

    if (v >= 8) {
        const int hv = v - 8;
        if (hv < 2) {
            __builtin_amdgcn_s_setprio(2);
            gemm1_loader<0>(hv, lane, p, mp, U0);
            __builtin_amdgcn_s_setprio(0);
        } else {
            const int ch = hv - 2, d = d0 + ch;
            unsigned pa[2], pi[2], pcr[2], pci[2];
            #pragma unroll
            for (int nh = 0; nh < 2; ++nh) { const int n = d * NS_ + 64 * nh + lane; pa[nh] = p.logA[n]; pi[nh] = p.Aim[n]; pcr[nh] = p.Cre[n]; pci[nh] = p.Cim[n]; }
            const unsigned pdt = p.logdt[d];
            BAR();
            const float dt = rbf(__expf(bf2f(pdt)));
            float ar2[2], th[2]; f32x2 c0[2];
            #pragma unroll
            for (int nh = 0; nh < 2; ++nh) {
                ar2[nh] = -rbf(__expf(bf2f(pa[nh]))) * dt * 1.4426950408889634f;
                th[nh] = bf2f(pi[nh]) * dt * 0.15915494309189535f;
                c0[nh] = f32x2{bf2f(pcr[nh]) * dt, bf2f(pci[nh]) * dt};
            }
            const float mx = wave_max(fmaxf(fmaxf(fabsf(c0[0].x), fabsf(c0[0].y)), fmaxf(fabsf(c0[1].x), fabsf(c0[1].y))));
            int e = (int)((__float_as_uint(mx) >> 23) & 0xFF);
            e = e < 10 ? 10 : (e > 240 ? 240 : e);
            const float scale2 = __uint_as_float((unsigned)(260 - e) << 23);
            if (lane == 0) lds_st<float>(L_SCL + 4 * ch, __uint_as_float((unsigned)(e - 5) << 23));
            const unsigned img = L_KG + ch * 4096;
            const unsigned wa = img + ((lane * 64) ^ (((lane >> 2) & 1) << 7)), wx = (lane >> 1) & 3;
            auto put16 = [&](const f32x2 (&z)[16], bool neg) {
                u32x4 re0, re1, im0, im1;
                #pragma unroll
                for (int i = 0; i < 4; ++i) {
                    re0[i] = pk_f16(z[2 * i].x, z[2 * i + 1].x); re1[i] = pk_f16(z[8 + 2 * i].x, z[9 + 2 * i].x);
                    im0[i] = neg ? pk_f16(-z[2 * i].y, -z[2 * i + 1].y) : pk_f16(z[2 * i].y, z[2 * i + 1].y);
                    im1[i] = neg ? pk_f16(-z[8 + 2 * i].y, -z[9 + 2 * i].y) : pk_f16(z[8 + 2 * i].y, z[9 + 2 * i].y);
                }
                lds_st<u32x4>(wa + 16 * (0 ^ wx), re0); lds_st<u32x4>(wa + 16 * (1 ^ wx), re1);
                lds_st<u32x4>(wa + 16 * (2 ^ wx), im0); lds_st<u32x4>(wa + 16 * (3 ^ wx), im1);
            };
            const int fq = lane >> 4, fe = (lane >> 2) & 3, fp = lane & 3;
            auto fraddr = [&](int ks, int t) { const int n = 16 * ks + 4 * fq + 2 * t + (fe >> 1); return img + ((n * 64) ^ (((n >> 2) & 1) << 7)) + 16 * ((2 * (fe & 1) + (fp >> 1)) ^ ((n >> 1) & 3)) + 8 * (fp & 1); };
            unsigned fa[2];
            #pragma unroll
            for (int t = 0; t < 2; ++t) fa[t] = fraddr(0, t);
            auto frag = [&](int ks) { return __builtin_bit_cast(f16x8, lds_tr2(fa[0] + 1024 * ks, fa[1] + 1024 * ks)); };
            f32x4 kacc[3] = {{0.f, 0.f, 0.f, 0.f}, {0.f, 0.f, 0.f, 0.f}, {0.f, 0.f, 0.f, 0.f}};
            BAR();
            #pragma unroll
            for (int nh = 0; nh < 2; ++nh) {
                const f32x2 z1 = cexp_p(ar2[nh], th[nh], 1.f), z16 = cexp_p(ar2[nh], th[nh], 16.f), z256 = cexp_p(ar2[nh], th[nh], 256.f);
                f32x2 z[16];
                z[0] = f32x2{1.f, 0.f}; z[1] = z16;
                #pragma unroll
                for (int c = 2; c < 16; ++c) z[c] = cmul(z[c - 1], z16);
                BAR();
                put16(z, false);
                f16x8 lo[4];
                #pragma unroll
                for (int ks = 0; ks < 4; ++ks) lo[ks] = frag(ks);
                const f32x2 cs = c0[nh] * scale2;
                z[0] = cs;
                #pragma unroll
                for (int i = 1; i < 16; ++i) z[i] = cmul(z[i - 1], z1);
                WAITL();
                #pragma unroll
                for (int ks = 0; ks < 4; ++ks) asm volatile("" : "+v"(lo[ks]));
                BAR();
                auto tile = [&](int t) {
                    #pragma unroll
                    for (int ks = 0; ks < 4; ++ks) kacc[t] = __builtin_amdgcn_mfma_f32_16x16x32_f16(frag(ks), lo[ks], kacc[t], 0, 0, 0);
                    asm volatile("" : "+v"(kacc[t]));
                };
                put16(z, true);
                const f32x2 z2 = cmul(z1, z1), z4 = cmul(z2, z2), z8 = cmul(z4, z4);
                f32x2 b = cmul(cs, z256);
                tile(0);
                BAR();
                z[0] = b;
                #pragma unroll
                for (int i = 1; i < 8; ++i) z[i] = cmul(z[i - 1], z2);
                #pragma unroll
                for (int a = 0; a < 2; ++a) { b = cmul(b, z256); z[8 + 4 * a] = b;
                    #pragma unroll
                    for (int i = 1; i < 4; ++i) z[8 + 4 * a + i] = cmul(z[8 + 4 * a + i - 1], z4); }
                WAITL();
                BAR();
                put16(z, true);
                #pragma unroll
                for (int a = 0; a < 4; ++a) { b = cmul(b, z256); z[2 * a] = b; z[2 * a + 1] = cmul(b, z8); }
                z[8] = cmul(b, z256);
                #pragma unroll
                for (int i = 9; i < 16; ++i) z[i] = f32x2{0.f, 0.f};
                tile(1);
                WAITL();
                BAR();
                put16(z, true);
                tile(2);
                WAITL();
                BAR();
            }
            {
                const int fr = lane & 15;
                #pragma unroll
                for (int t = 0; t < 3; ++t)
                    #pragma unroll
                    for (int r = 0; r < 4; ++r) {
                        const int i = 4 * fq + r, c = fr;
                        int idx;
                        if (t == 0) idx = 16 * c + i;
                        else if (t == 1) idx = i < 8 ? 256 + 8 * c + i : (i < 12 ? 384 + 4 * c + (i - 8) : 448 + 4 * c + (i - 12));
                        else idx = i < 8 ? 512 + 32 * (i >> 1) + 2 * c + (i & 1) : ((i == 8 && c == 0) ? 640 : 644);
                        lds_st<_Float16>(L_KP + ch * 1296 + idx * 2, (_Float16)kacc[t][r]);
                    }
            }
            BARL();
        }
        STAMP(8);
        const unsigned tag = lds_ld<unsigned>(L_SCL + 8);
        #pragma unroll
        for (int i = 0; i < 4; ++i) wait_flags(ws, WS_FLAGX + 256 * i, 64, tag, lane);
        if (w == 0 && hv == 0 && lane == 0) st_sc1_u32(ws + WS_EPOCH, tag);
        const unsigned L_RAW = L_CRED;
        {
            const int ch = lane & 1;
            u32x4 raw[2];
            #pragma unroll
            for (int i = 0; i < 2; ++i) { const int tbk = 64 * hv + 32 * i + (lane >> 1); raw[i] = __builtin_amdgcn_raw_buffer_load_b128(rs_ws, WS_XNT + (d0 >> 3) * 32768 + tbk * 128 + ((d0 & 7) + ch) * 16, 0, AUX_SC1); }
            #pragma unroll
            for (int c2 = 0; c2 < 2; ++c2) {
                const int qq = 64 * hv + lane, a = 255 - qq;
                const unsigned kp = L_KP + c2 * 1296;
                u32x4 o;
                if (hv < 2) {
                    const unsigned A = lds_ld<u16>(kp + 2 * (512 + a - 128)), B = lds_ld<u16>(kp + 2 * (513 + a - 128));
                    const unsigned AA = A | (A << 16), BB = B | (B << 16);
                    o = u32x4{BB, (a & 1) ? BB : (B | (A << 16)), AA, AA};
                } else if (hv == 2) {
                    const unsigned A = lds_ld<u16>(kp + 2 * (384 + 2 * (a - 64))), B = lds_ld<u16>(kp + 2 * (385 + 2 * (a - 64))), C = lds_ld<u16>(kp + 2 * (386 + 2 * (a - 64)));
                    o = u32x4{C | (C << 16), B | (B << 16), B | (A << 16), A | (A << 16)};
                } else if (a >= 32) {
                    const u32x2 ab = lds_ld<u32x2>(kp + 2 * (256 + 4 * (a - 32)));
                    const unsigned E = lds_ld<u16>(kp + 2 * (260 + 4 * (a - 32)));
                    const unsigned A = ab[0] & 0xFFFFu, B = ab[0] >> 16, C = ab[1] & 0xFFFFu, Dd = ab[1] >> 16;
                    o = u32x4{E | (Dd << 16), C | (C << 16), C | (B << 16), A | (A << 16)};
                } else {
                    const u32x4 t = lds_ld<u32x4>(kp + 16 * a);
                    o = u32x4{__builtin_amdgcn_alignbit(t[3], t[3], 16), __builtin_amdgcn_alignbit(t[2], t[2], 16), __builtin_amdgcn_alignbit(t[1], t[1], 16), __builtin_amdgcn_alignbit(t[0], t[0], 16)};
                }
                lds_st<u32x4>(L_KR + c2 * KRB + 16 * (qq ^ ((qq >> 4) & 3)), o);
                const int qz = 256 + 31 * hv + (lane < 31 ? lane : 30);
                lds_st<u32x4>(L_KR + c2 * KRB + 16 * (qz ^ ((qz >> 4) & 3)), u32x4{0u, 0u, 0u, 0u});
            }
            #pragma unroll
            for (int i = 0; i < 2; ++i) lds_st<u32x4>(L_RAW + ch * 4160 + 16 * (4 + 64 * hv + 32 * i + (lane >> 1)), raw[i]);
            if (hv == 0 && lane < 8) lds_st<u32x4>(L_RAW + (lane & 1) * 4160 + 16 * (lane >> 1), u32x4{0u, 0u, 0u, 0u});
        }
        grp_bar(L_CNT + 4, 4, lane);
        {
            const int ht = tid - 512;
            #pragma unroll
            for (int ch = 0; ch < 2; ++ch) {
                auto copies = [&](int b) {
                    const u32x4 c = lds_ld<u32x4>(L_RAW + ch * 4160 + 16 * b);
                    u32x4 n = {0u, 0u, 0u, 0u};
                    if (b < 259) n = lds_ld<u32x4>(L_RAW + ch * 4160 + 16 * (b + 1));
                    const unsigned ub = L_U + ch * 4 * UCP + 16 * b;
                    lds_st<u32x4>(ub, u32x4{__builtin_amdgcn_alignbyte(c[1], c[0], 2), __builtin_amdgcn_alignbyte(c[2], c[1], 2), __builtin_amdgcn_alignbyte(c[3], c[2], 2), __builtin_amdgcn_alignbyte(n[0], c[3], 2)});
                    lds_st<u32x4>(ub + UCP, u32x4{c[1], c[2], c[3], n[0]});
                    lds_st<u32x4>(ub + 2 * UCP, u32x4{__builtin_amdgcn_alignbyte(c[2], c[1], 2), __builtin_amdgcn_alignbyte(c[3], c[2], 2), __builtin_amdgcn_alignbyte(n[0], c[3], 2), __builtin_amdgcn_alignbyte(n[1], n[0], 2)});
                    lds_st<u32x4>(ub + 3 * UCP, u32x4{c[2], c[3], n[0], n[1]});
                };
                copies(ht + 4);
                if (ht < 4) copies(ht);
            }
        }
        grp_bar(L_CNT + 4, 8, lane);
        STAMP(9);
        {
            const int ch = hv >> 1, half = hv & 1;
            f32x16 y0 = {}, y1 = {};
            const unsigned a_u = L_U + ch * 4 * UCP + q4 * UCP + 2 * (8 * hh + 16 * gm + 4 * p4) + 1024 * half;
            const int qb = 252 - 4 * r32 + 64 * half + hh;
            auto rdk = [&](int q) { return lds_ld<s16x8>(L_KR + ch * KRB + 16 * (q ^ ((q >> 4) & 3))); };
            s16x8 Ua = lds_tr2(a_u, a_u + 8), Ub = lds_tr2(a_u + 2048, a_u + 2056), K0 = rdk(qb), K1 = rdk(qb - 128);
            #pragma unroll 8
            for (int i = 0; i < 32; ++i) {
                const int in = i < 31 ? i + 1 : 31;
                y0 = MFMA_H(Ua, K0, y0);
                SB0();
                const s16x8 nUa = lds_tr2(a_u + 32 * in, a_u + 32 * in + 8);
                const s16x8 nK1 = rdk(qb + 2 * in - 128);
                SB0();
                y1 = MFMA_H(Ua, K1, y1);
                SB0();
                const s16x8 nUb = lds_tr2(a_u + 32 * in + 2048, a_u + 32 * in + 2056);
                SB0();
                y1 = MFMA_H(Ub, K0, y1);
                SB0();
                const s16x8 nK0 = rdk(qb + 2 * in);
                Ua = nUa; Ub = nUb; K0 = nK0; K1 = nK1;
                SB0();
            }
            STAMP(10);
            const unsigned rb = L_CRED + ch * CRED_W + r32 * 144 + hh * 16;
            #pragma unroll
            for (int gq = 0; gq < 4; ++gq) {
                if (half) lds_st<f32x4>(rb + gq * 32, f32x4{y0[4 * gq], y0[4 * gq + 1], y0[4 * gq + 2], y0[4 * gq + 3]});
                else lds_st<f32x4>(rb + 32 * 144 + gq * 32, f32x4{y1[4 * gq], y1[4 * gq + 1], y1[4 * gq + 2], y1[4 * gq + 3]});
            }
            const float isc = lds_ld<float>(L_SCL + 4 * ch);
            const float dsk = bf2f(p.Dskip[d0 + ch]);
            grp_bar(L_CNT + 4, 12, lane);
            const unsigned ob = rb + (half ? 32 * 144 : 0);
            #pragma unroll
            for (int gq = 0; gq < 4; ++gq) {
                const f32x4 o = lds_ld<f32x4>(ob + gq * 32);
                const int l0 = 32 * (r32 + 32 * half) + 8 * gq + 4 * hh;
                const unsigned ua = L_U + ch * 4 * UCP + 3 * UCP + 2 * (l0 + 28);
                const f16x2 u01 = lds_ld<f16x2>(ua), u23 = lds_ld<f16x2>(ua + 4);
                const float uu[4] = {(float)u01[0], (float)u01[1], (float)u23[0], (float)u23[1]};
                float r[4];
                #pragma unroll
                for (int i = 0; i < 4; ++i) { const float y = rbf(((half ? y1[4 * gq + i] : y0[4 * gq + i]) + o[i]) * isc); r[i] = y + rbf(uu[i] * dsk); }
                __builtin_amdgcn_raw_buffer_store_b64(u32x2{pk_bf(r[0], r[1]), pk_bf(r[2], r[3])}, rs_ws, WS_AT + (d0 + ch) * 4096 + l0 * 2, 0, AUX_SC1);
            }
            WAITV(0);
            grp_bar(L_CNT + 4, 16, lane);
            if (hv == 0 && lane == 0) st_sc1_u32(ws + WS_FLAGA + 4 * (d0 >> 1), tag);
        }
        STAMP(11);
        {
            const rsrc_t rs_wd = mk_rsrc(p.wd, D_ * F_ * 2);
            const unsigned vo_d = (lane >> 3) * 1024 + 16 * ((lane & 7) ^ (((lane >> 4) & 1) << 2));
            auto stage2 = [&](int st) {
                #pragma unroll
                for (int e = 0; e < 4; ++e) {
                    const int U = 8 * st + 2 * hv + (e >> 1);
                    dma16(G2S[st % 7] + (2 * hv + (e >> 1)) * 2048 + (e & 1) * 1024, vo_d, rs_wd, (unsigned)((16 * U + 8 * (e & 1)) * 1024 + 128 * ct));
                }
            };
            stage2(4); stage2(5); stage2(6);
            #pragma unroll
            for (int st = 3; st < 10; ++st) {
                if (st == 3) WAITV(8); else if (st <= 6) WAITV(8); else if (st == 7) WAITV(8); else if (st == 8) WAITV(4); else WAITV(0);
                BAR();
                if (st + 4 < 11) stage2(st + 4);
            }
        }
        BARL(); BARL(); BARL();
        STAMP(16); STAMPC(31);
        STAMP_DUMP();
        return;
    }

    const int tb = v & 3, ug = v >> 2;
    unsigned tag;
    {
        const u32x4 xrow = *(const GAS u32x4*)((const unsigned char*)p.x + (size_t)(8 * tb8 + v) * 1024 + lane * 16);
        tag = __builtin_amdgcn_readfirstlane(ld_sc1_u32(ws + WS_EPOCH)) + 1u;
        if (tid == 0) lds_st<unsigned>(L_SCL + 8, tag);
        f32x2 xv[4];
        #pragma unroll
        for (int i = 0; i < 4; ++i) xv[i] = f32x2{bf2f(xrow[i] & 0xFFFFu), __uint_as_float(xrow[i] & 0xFFFF0000u)};
        const f32x2 s2 = (xv[0] + xv[1]) + (xv[2] + xv[3]);
        const float mean = wave_sum(s2.x + s2.y) * (1.f / 512.f);
        f32x2 q2 = {0.f, 0.f};
        #pragma unroll
        for (int i = 0; i < 4; ++i) { const f32x2 dd = xv[i] - mean; q2 = dd * dd + q2; }
        const float var = wave_sum(q2.x + q2.y) * (1.f / 512.f);
        const float mb = rbf(mean), sb = rbf(__builtin_amdgcn_sqrtf(rbf(rbf(var) + 1e-5f)));
        const float ri = __builtin_amdgcn_rcpf(sb);
        u32x4 o;
        #pragma unroll
        for (int i = 0; i < 4; ++i) {
            const f32x2 dd = xv[i] - mb;
            const unsigned a = pk_bf(dd.x, dd.y);
            const f32x2 af = {bf2f(a & 0xFFFFu), __uint_as_float(a & 0xFFFF0000u)};
            f32x2 qq = af * ri;
            qq = (af - qq * sb) * ri + qq;
            const unsigned b = pk_bf(qq.x, qq.y);
            o[i] = pk_f16(bf2f(b & 0xFFFFu), __uint_as_float(b & 0xFFFF0000u));
        }
        lds_st<u32x4>(L_LN + v * 1024 + lane * 16, o);
        if (lane == 0) {
            const unsigned long long gr = ((unsigned long long)tag << 32) | (f2bf(mb) | (f2bf(sb) << 16));
            __hip_atomic_store((GAS unsigned long long*)(ws + WS_STATS + (8 * tb8 + v) * 8), gr, RLX_AGENT);
        }
        STAMP(1);
        grp_bar(L_CNT, 8, lane);
        #pragma unroll
        for (int i = 0; i < 4; ++i) {
            const unsigned a = lds_ld<u16>(L_LN + (2 * i) * 1024 + tid * 2), b = lds_ld<u16>(L_LN + (2 * i + 1) * 1024 + tid * 2);
            o[i] = a | (b << 16);
        }
        __builtin_amdgcn_raw_buffer_store_b128(o, rs_ws, WS_XNT + (tid >> 3) * 32768 + tb8 * 128 + (tid & 7) * 16, 0, AUX_SC1);
    }
    STAMP(3);
    BARL();
    STAMP(4);

    f32x16 acc[3] = {};
    float cs[3] = {0.f, 0.f, 0.f};
    unsigned long long gran = 0;
    const bool full = ug == 0 || nunits == 6;
    const unsigned stat_off = WS_STATS + (128 * mp + 32 * tb + r32) * 8;
    if (full) gemm1_loop<3>(acc, cs, gran, tb, ug, lane, tid, tag, ws, WS_FLAGX + 4 * tb8, stat_off);
    else gemm1_loop<1>(acc, cs, gran, tb, ug, lane, tid, tag, ws, WS_FLAGX + 4 * tb8, stat_off);
    STAMP(5);
    #pragma unroll
    for (int uu = 0; uu < 3; ++uu) lds_st<float>(L_PART + ((3 * ug + uu) * 32 + r32) * 32 + (2 * tb + hh) * 4, cs[uu]);
    WAITV(0);
    asm volatile("" : "+v"(gran));
    for (unsigned spins = 0; !__all((unsigned)(gran >> 32) == tag);) {
        if (spin_fail(spins, ws, tag)) break;
        gran = __hip_atomic_load((const GAS unsigned long long*)(ws + stat_off), RLX_AGENT);
    }
    const float t_mean = bf2f((unsigned)gran & 0xFFFFu), t_ri = __builtin_amdgcn_rcpf(__uint_as_float((unsigned)gran & 0xFFFF0000u));
    grp_bar(L_CNT, 16, lane);
    {
        const unsigned nm = pk_bf(-t_mean, -t_mean);
        const s16x8 Bm = __builtin_bit_cast(s16x8, u32x4{nm, nm, nm, nm});
        #pragma unroll
        for (int uu = 0; uu < 3; ++uu) {
            if (uu == 0 || full) {
                const unsigned pa = L_PART + ((3 * ug + uu) * 32 + r32) * 32;
                const f32x4 p0 = lds_ld<f32x4>(pa), p1 = lds_ld<f32x4>(pa + 16);
                u32x4 aw;
                #pragma unroll
                for (int i = 0; i < 4; ++i) {
                    const float a = i < 2 ? p0[2 * i] : p1[2 * i - 4], b = i < 2 ? p0[2 * i + 1] : p1[2 * i - 3];
                    const unsigned hi = pk_bf(a, b);
                    const unsigned lo = pk_bf(a - bf2f(hi & 0xFFFFu), b - __uint_as_float(hi & 0xFFFF0000u));
                    aw[i] = hh ? lo : hi;
                }
                acc[uu] = MFMA_BF(__builtin_bit_cast(s16x8, aw), Bm, acc[uu]);
            }
        }
        #pragma unroll
        for (int uu = 0; uu < 3; ++uu) {
            if (uu == 0 || full) {
                u32x4 o;
                #pragma unroll
                for (int i = 0; i < 4; ++i) {
                    const f32x2 gv = f32x2{acc[uu][2 * i], acc[uu][2 * i + 1]} * t_ri, uv = f32x2{acc[uu][8 + 2 * i], acc[uu][9 + 2 * i]} * t_ri;
                    const f32x2 zz = gv * (gv * gv * -0.10294324f + -2.3022082f);
                    const f32x2 den = f32x2{__builtin_amdgcn_exp2f(zz.x), __builtin_amdgcn_exp2f(zz.y)} + 1.f;
                    const f32x2 hv = gv * uv * f32x2{__builtin_amdgcn_rcpf(den.x), __builtin_amdgcn_rcpf(den.y)};
                    o[i] = pk_bf(hv.x, hv.y);
                }
                __builtin_amdgcn_raw_buffer_store_b128(o, rs_ws, WS_H + ((4 * mp + tb) * 86 + U0 + 3 * ug + uu) * 1024 + lane * 16, 0, AUX_SC1);
            }
        }
    }
    STAMP(6);

    {
        const int th = v & 1, kq = v >> 1;
        const rsrc_t rs_wd = mk_rsrc(p.wd, D_ * F_ * 2);
        const unsigned vo_d = (lane >> 3) * 1024 + 16 * ((lane & 7) ^ (((lane >> 4) & 1) << 2));
        WAITV(0);
        grp_bar(L_CNT, 24, lane);
        if (tid == 0) st_sc1_u32(ws + WS_FLAGH + mp * 64 + 4 * ng, tag);
        STAMP(7);
        #pragma unroll
        for (int st = 0; st < 4; ++st) {
            const int U = 8 * st + v;
            dma16(G2S[st] + v * 2048, vo_d, rs_wd, (unsigned)(16 * U * 1024 + 128 * ct));
            dma16(G2S[st] + v * 2048 + 1024, vo_d, rs_wd, (unsigned)((16 * U + 8) * 1024 + 128 * ct));
        }
        const int tk = tid >> 3, ck = tid & 7;
        const u32x4 xv = *(const GAS u32x4*)((const unsigned char*)p.x + (size_t)(64 * rt + tk) * 1024 + (64 * ct + 8 * ck) * 2);
        wait_flags(ws, WS_FLAGH + (rt >> 1) * 64, 16, tag, lane);
        STAMP(12);
        const unsigned hbase = WS_H + (2 * rt + th) * 86 * 1024 + kq * 1024;
        u32x4 hf[22];
        const unsigned vl16 = lane * 16;
        auto ldh = [&](int i) {
            const int U = 8 * (i >> 1) + 4 * (i & 1);
            const unsigned so = (U + 3 < 86 || kq < 2) ? hbase + U * 1024 : hbase;
            asm volatile("buffer_load_dwordx4 %0, %1, %2, %3 offen sc1" : "=v"(hf[i]) : "v"(vl16), "s"(rs_ws), "s"(so) : "memory");
        };
        constexpr int HD = 12;
        #pragma unroll
        for (int i = 0; i < HD; ++i) ldh(i);
        f32x16 d0a = {}, d1a = {};
        const unsigned sw = ((q4 >> 1) & 1) << 2;
        unsigned bd[2][2];
        #pragma unroll
        for (int b = 0; b < 2; ++b)
            #pragma unroll
            for (int nb = 0; nb < 2; ++nb) { bd[b][nb] = kq * 2048 + (4 * hh + q4) * 128 + 16 * ((4 * nb + 2 * gm + (p4 >> 1)) ^ sw) + 8 * (p4 & 1) + b * 65536; asm volatile("" : "+v"(bd[b][nb])); }
        auto rdd = [&](int i, int nb) { const unsigned s = G2S[(i >> 1) % 7] + (i & 1) * 8192; const unsigned a = bd[s >> 16][nb] + (s & 0xFFFFu); return lds_tr2(a, a + 1024); };
        WAITV(HD);
        grp_bar(L_CNT, 32, lane);
        STAMP(13);
        s16x8 W0 = rdd(0, 0), W1 = rdd(0, 1);
        #pragma unroll
        for (int st = 0; st < 11; ++st) {
            if (st >= 3 && st < 10) BAR();
            #pragma unroll
            for (int e = 0; e < 2; ++e) {
                const int i = 2 * st + e;
                if (i + HD < 22) ldh(i + HD);
                const int rem = (i + HD < 22) ? HD : 21 - i;
                asm volatile("s_waitcnt vmcnt(%1)" : "+v"(hf[i]) : "i"(rem) : "memory");
                const s16x8 H = __builtin_bit_cast(s16x8, hf[i]);
                s16x8 N0 = W0, N1 = W1;
                if (i < 21 || kq < 2) {
                    d0a = MFMA_BF(W0, H, d0a);
                    SB0();
                    if (i + 1 < 22) N0 = rdd(i + 1, 0);
                    SB0();
                    d1a = MFMA_BF(W1, H, d1a);
                    SB0();
                    if (i + 1 < 22) N1 = rdd(i + 1, 1);
                }
                W0 = N0; W1 = N1;
                SB0();
            }
        }
        STAMP(14);
        wait_flags(ws, WS_FLAGA + 4 * 32 * ct, 32, tag, lane);
        const u32x4 at = __builtin_amdgcn_raw_buffer_load_b128(rs_ws, WS_AT + (64 * ct + (tid >> 3)) * 4096 + (64 * rt + 8 * (tid & 7)) * 2, 0, AUX_SC1);
        BARL();
        const unsigned rb = L_RED + kq * RED_W + (32 * th + r32) * 272 + hh * 16;
        #pragma unroll
        for (int gq = 0; gq < 4; ++gq) {
            lds_st<f32x4>(rb + gq * 32, f32x4{d0a[4 * gq], d0a[4 * gq + 1], d0a[4 * gq + 2], d0a[4 * gq + 3]});
            lds_st<f32x4>(rb + 128 + gq * 32, f32x4{d1a[4 * gq], d1a[4 * gq + 1], d1a[4 * gq + 2], d1a[4 * gq + 3]});
        }
        #pragma unroll
        for (int i = 0; i < 4; ++i) lds_st<unsigned>(L_ATT + (tid >> 3) * 132 + (tid & 7) * 16 + 4 * i, at[i]);
        BARL();
        STAMP(15);
        f32x4 s0 = {0.f, 0.f, 0.f, 0.f}, s1 = s0;
        #pragma unroll
        for (int q = 0; q < 4; ++q) {
            const unsigned a = L_RED + q * RED_W + tk * 272 + ck * 32;
            s0 += lds_ld<f32x4>(a); s1 += lds_ld<f32x4>(a + 16);
        }
        float o[8];
        #pragma unroll
        for (int i = 0; i < 8; ++i) {
            const float av = bf2f(lds_ld<u16>(L_ATT + (8 * ck + i) * 132 + tk * 2));
            const float xx = (i & 1) ? __uint_as_float(xv[i >> 1] & 0xFFFF0000u) : bf2f(xv[i >> 1] & 0xFFFFu);
            o[i] = rbf(xx + av) + rbf(i < 4 ? s0[i & 3] : s1[i & 3]);
        }
        const u32x4 ov = {pk_bf(o[0], o[1]), pk_bf(o[2], o[3]), pk_bf(o[4], o[5]), pk_bf(o[6], o[7])};
        __builtin_amdgcn_raw_buffer_store_b128(ov, mk_rsrc(p.out, L_ * D_ * 2), (64 * rt + tk) * 1024 + (64 * ct + 8 * ck) * 2, 0, AUXO);
        BARL();
    }
    STAMP(16); STAMPC(31);
    STAMP_DUMP();
}

extern "C" void kernel_launch(void* const* d_in, const int* in_sizes, int n_in, void* d_out, int out_size, void* d_ws, size_t ws_size, hipStream_t stream) {
    (void)in_sizes; (void)n_in; (void)out_size; (void)ws_size;
    Params p;
    p.x = (const u16*)d_in[0]; p.logA = (const u16*)d_in[1]; p.Aim = (const u16*)d_in[2]; p.Cre = (const u16*)d_in[3]; p.Cim = (const u16*)d_in[4];
    p.logdt = (const u16*)d_in[5]; p.Dskip = (const u16*)d_in[6]; p.wg = (const u16*)d_in[8]; p.wu = (const u16*)d_in[9]; p.wd = (const u16*)d_in[10];
    p.out = (u16*)d_out; p.ws = (unsigned char*)d_ws;
    (void)hipFuncSetAttribute((const void*)fused_kernel, hipFuncAttributeMaxDynamicSharedMemorySize, LDS_BYTES);
    fused_kernel<<<256, 768, LDS_BYTES, stream>>>(p);
}
```

```cpp
#include <hip/hip_runtime.h>
#include <stdint.h>

#define LAS __attribute__((address_space(3)))
#define GAS __attribute__((address_space(1)))
#define DI __device__ __forceinline__
typedef unsigned short u16;
typedef short s16x4 __attribute__((ext_vector_type(4)));
typedef short s16x8 __attribute__((ext_vector_type(8)));
typedef _Float16 f16x8 __attribute__((ext_vector_type(8)));
typedef _Float16 f16x2 __attribute__((ext_vector_type(2)));
typedef __bf16 bf16x2 __attribute__((ext_vector_type(2)));
typedef float f32x2 __attribute__((ext_vector_type(2)));
typedef float f32x4 __attribute__((ext_vector_type(4)));
typedef float f32x16 __attribute__((ext_vector_type(16)));
typedef unsigned u32x2 __attribute__((ext_vector_type(2)));
typedef unsigned u32x4 __attribute__((ext_vector_type(4)));
typedef __amdgpu_buffer_rsrc_t rsrc_t;

constexpr int L_ = 2048, D_ = 512, NS_ = 128, F_ = 1376;
constexpr unsigned WS_EPOCH = 0, WS_TMO = 256, WS_FLAGX = 1024, WS_FLAGH = 4096, WS_FLAGA = 8192, WS_FLAGC = 12288, WS_STATS = 20480;
constexpr unsigned WS_XNT = 65536, WS_H = WS_XNT + 2097152, WS_AT = WS_H + 64 * 86 * 1024, WS_CS = WS_AT + 2097152, WS_END = WS_CS + 16 * 6 * 2048;
constexpr unsigned WS_STAMP = 16u << 20;
constexpr unsigned XSLOT = 16384, W_RING = 65536, WSLOT = 12352, W_M = 6176, MISC = W_RING + 6 * WSLOT;
constexpr unsigned L_KP = MISC;
constexpr unsigned L_SCL = L_KP + 2592;
constexpr unsigned L_CNT = L_SCL + 16;
constexpr unsigned C_M = L_CNT, C_H = L_CNT + 4, C_GELU = L_CNT + 8, C_HF = L_CNT + 12;
constexpr unsigned L_LN = L_CNT + 64;
constexpr unsigned KGW = 4352, L_KG = 98304;
constexpr unsigned L_STAMP = L_LN + 18432;
constexpr unsigned L_END = L_STAMP + 768;
static_assert(L_END <= 163840, "lds");
constexpr unsigned UCP = 4160, L_U = 0;
constexpr unsigned KRS = 1568, KRB = 4 * KRS, L_KR = W_RING + 4 * WSLOT + 1024;
constexpr unsigned L_ULAST = L_KR + 2 * KRB + 192;
constexpr unsigned CRED_W = 64 * 144, L_CRED = L_LN;
static_assert(L_KG + 4 * KGW <= L_KR && (L_KR & 255) == 0, "images");
static_assert(L_U + 7 * UCP <= 2 * XSLOT && L_ULAST + UCP <= MISC && ((L_U + 7 * UCP) & 255) == (L_ULAST & 255) && L_CRED + 2 * CRED_W <= L_STAMP, "conv lds");
constexpr unsigned G2S[11] = {32768, 49152, 65536, 81920, 98304, 0, 16384, L_KR, L_CRED, 32768, 49152};
static_assert(98304 + 16384 <= L_KR && L_KR + 16384 <= MISC, "g2 slots");
constexpr unsigned L_RED = 0, RED_W = 64 * 272, L_ATT = 4 * RED_W;
static_assert(L_ATT + 64 * 132 <= MISC, "g2 lds");
constexpr int LDS_BYTES = 163840;
constexpr unsigned SPIN_CAP = 1u << 16;
constexpr int AUX_SC1 = 16;
#define AUX_OUT 17
#define ROT (2 * (mp & 7))

struct Params {
    const u16* x; const u16* logA; const u16* Aim; const u16* Cre; const u16* Cim; const u16* logdt; const u16* Dskip;
    const u16* wg; const u16* wu; const u16* wd; u16* out; unsigned char* ws;
};

DI float bf2f(unsigned b) { return __uint_as_float(b << 16); }
DI unsigned pk_bf(float a, float b) { f32x2 v = {a, b}; return __builtin_bit_cast(unsigned, __builtin_convertvector(v, bf16x2)); }
DI unsigned f2bf(float f) { return pk_bf(f, 0.f) & 0xFFFFu; }
DI unsigned pk_f16(float a, float b) { f32x2 v = {a, b}; return __builtin_bit_cast(unsigned, __builtin_convertvector(v, f16x2)); }
DI float rbf(float f) { return bf2f(f2bf(f)); }
#define DPPF(v, ctrl) __builtin_bit_cast(float, __builtin_amdgcn_update_dpp(0, __builtin_bit_cast(int, (v)), (ctrl), 0xF, 0xF, true))
DI float rdl(float v, int l) { return __builtin_bit_cast(float, __builtin_amdgcn_readlane(__builtin_bit_cast(int, v), l)); }
DI float wave_sum(float v) {
    v += DPPF(v, 0xB1); v += DPPF(v, 0x4E); v += DPPF(v, 0x141); v += DPPF(v, 0x140);
    return (rdl(v, 0) + rdl(v, 16)) + (rdl(v, 32) + rdl(v, 48));
}
DI float wave_max(float v) {
    v = fmaxf(v, DPPF(v, 0xB1)); v = fmaxf(v, DPPF(v, 0x4E)); v = fmaxf(v, DPPF(v, 0x141)); v = fmaxf(v, DPPF(v, 0x140));
    return fmaxf(fmaxf(rdl(v, 0), rdl(v, 16)), fmaxf(rdl(v, 32), rdl(v, 48)));
}
DI rsrc_t mk_rsrc(const void* p, unsigned bytes) { return __builtin_amdgcn_make_buffer_rsrc((void*)p, 0, bytes, 0x00020000); }
DI void dma16(unsigned lds, unsigned voff, rsrc_t rs, unsigned soff) {
    asm volatile("s_mov_b32 m0, %0\n\ts_nop 0\n\tbuffer_load_dwordx4 %1, %2, %3 offen lds" :: "s"(lds), "v"(voff), "s"(rs), "s"(soff) : "memory");
}
#define WAITV(n) asm volatile("s_waitcnt vmcnt(%0)" :: "i"(n) : "memory")
#define WAITL() asm volatile("s_waitcnt lgkmcnt(0)" ::: "memory")
#define SB0() __builtin_amdgcn_sched_barrier(0)
#define BAR() do { SB0(); asm volatile("s_barrier" ::: "memory"); SB0(); } while (0)
#define BARL() do { SB0(); asm volatile("s_waitcnt lgkmcnt(0)\n\ts_barrier" ::: "memory"); SB0(); } while (0)
template <class T> DI T lds_ld(unsigned a) { return *(const LAS T*)(size_t)a; }
template <class T> DI void lds_st(unsigned a, T v) { *(LAS T*)(size_t)a = v; }
DI s16x8 lds_tr2(unsigned a0, unsigned a1) {
    s16x4 lo = __builtin_amdgcn_ds_read_tr16_b64_v4i16((LAS s16x4*)(size_t)a0);
    s16x4 hi = __builtin_amdgcn_ds_read_tr16_b64_v4i16((LAS s16x4*)(size_t)a1);
    return __builtin_shufflevector(lo, hi, 0, 1, 2, 3, 4, 5, 6, 7);
}
DI void grp_bar(unsigned cnt, unsigned target, int lane) {
    SB0();
    asm volatile("s_waitcnt lgkmcnt(0)" ::: "memory");
    if (lane == 0) { const unsigned one = 1; asm volatile("ds_add_u32 %0, %1" :: "v"(cnt), "v"(one) : "memory"); }
    for (unsigned it = 0; it < (1u << 22); ++it) {
        unsigned c;
        asm volatile("ds_read_b32 %0, %1\n\ts_waitcnt lgkmcnt(0)" : "=v"(c) : "v"(cnt) : "memory");
        if (__builtin_amdgcn_readfirstlane(c) >= target) break;
    }
    SB0();
}
#define MFMA_BF(a, b, c) __builtin_amdgcn_mfma_f32_32x32x16_bf16((a), (b), (c), 0, 0, 0)
#define MFMA_H(a, b, c) __builtin_amdgcn_mfma_f32_32x32x16_f16(__builtin_bit_cast(f16x8, a), __builtin_bit_cast(f16x8, b), (c), 0, 0, 0)
#define RLX_AGENT __ATOMIC_RELAXED, __HIP_MEMORY_SCOPE_AGENT
DI unsigned ld_sc1_u32(const unsigned char* p) { return __hip_atomic_load((const GAS unsigned*)p, RLX_AGENT); }
DI void st_sc1_u32(unsigned char* p, unsigned v) { __hip_atomic_store((GAS unsigned*)p, v, RLX_AGENT); }
DI bool spin_fail(unsigned& spins, unsigned char* ws, unsigned tag) {
    __builtin_amdgcn_s_sleep(1);
    if (++spins > SPIN_CAP) { st_sc1_u32(ws + WS_TMO, tag); return true; }
    return (spins & 255u) == 0u && __builtin_amdgcn_readfirstlane(ld_sc1_u32(ws + WS_TMO)) == tag;
}
DI void wait_flags(unsigned char* ws, unsigned off, int nw, unsigned tag, int lane) {
    for (unsigned spins = 0;;) {
        unsigned v = tag;
        if (lane < nw) v = ld_sc1_u32(ws + off + 4 * lane);
        if (__all(v == tag)) return;
        if (spin_fail(spins, ws, tag)) return;
    }
}
DI void lds_arrive(unsigned cnt) {
    const unsigned one = 1;
    asm volatile("s_mov_b64 exec, 1\n\tds_add_u32 %0, %1\n\ts_mov_b64 exec, -1" :: "v"(cnt), "v"(one) : "memory");
}
DI void lds_wait_ge(unsigned cnt, unsigned target) {
    SB0();
    for (unsigned it = 0; it < (1u << 22); ++it) {
        unsigned c;
        asm volatile("ds_read_b32 %0, %1\n\ts_waitcnt lgkmcnt(0)" : "=v"(c) : "v"(cnt) : "memory");
        if (__builtin_amdgcn_readfirstlane(c) >= target) break;
        __builtin_amdgcn_s_sleep(1);
    }
    SB0();
}
#define STAMP(i) do { } while (0)
#define STAMPC(i) do { } while (0)
#define STAMP_DUMP() do { } while (0)

#define TICK_DECL2
#define TICK2(i) do { } while (0)
#define TICK2_DUMP(n, blk) do { } while (0)
#define TICK3(i) do { } while (0)
#define TICK_DECL
#define TICK(i) do { } while (0)
#define TICK_DUMP(n, blk) do { } while (0)
DI bool all_eq_hp(unsigned a, unsigned b_uniform) {
    unsigned long long m;
    asm volatile("s_setprio 1\n\tv_cmp_ne_u32 %0, %1, %2\n\ts_setprio 0" : "=s"(m) : "s"(b_uniform), "v"(a));
    return m == 0;
}
typedef f32x2 C2;
DI void cmul2(C2& r0, const C2 a0, const C2 b0, C2& r1, const C2 a1, const C2 b1) {
    float x0, y0, x1, y1;
    const float a0x = a0.x, a0y = a0.y, b0x = b0.x, b0y = b0.y, a1x = a1.x, a1y = a1.y, b1x = b1.x, b1y = b1.y;
    asm volatile("v_mul_f32 %0, %4, %6\n\tv_mul_f32 %1, %4, %7\n\tv_mul_f32 %2, %8, %10\n\tv_mul_f32 %3, %8, %11\n\t"
        "v_fma_f32 %0, -%5, %7, %0\n\tv_fmac_f32 %1, %5, %6\n\tv_fma_f32 %2, -%9, %11, %2\n\tv_fmac_f32 %3, %9, %10"
        : "=&v"(x0), "=&v"(y0), "=&v"(x1), "=&v"(y1)
        : "v"(a0x), "v"(a0y), "v"(b0x), "v"(b0y), "v"(a1x), "v"(a1y), "v"(b1x), "v"(b1y));
    r0 = C2{x0, y0}; r1 = C2{x1, y1};
}
DI C2 cexp_p(float ar2, float th, float p) {
    const float m = __builtin_amdgcn_exp2f(ar2 * p), t = __builtin_amdgcn_fractf(th * p);
    const float c = __builtin_amdgcn_cosf(t), s = __builtin_amdgcn_sinf(t);
    float x, y;
    asm volatile("s_nop 0\n\tv_mul_f32 %0, %2, %3\n\tv_mul_f32 %1, %2, %4" : "=&v"(x), "=&v"(y) : "v"(m), "v"(c), "v"(s));
    return C2{x, y};
}

template <int NU>
DI void gemm1_loop(f32x16 (&acc)[3], u32x4 (&csf)[3][2], unsigned long long& gran, int v, int tb, int ug, int mp, int ng, int lane, unsigned tag, unsigned char* ws, rsrc_t rs_ws, unsigned stat_off) {
    const int r32 = lane & 31, hh = lane >> 5, G = lane >> 4, q4 = (lane >> 2) & 3, p4 = lane & 3, gm = G & 1;
    unsigned bx[4], bw[2];
    #pragma unroll
    for (int ks = 0; ks < 4; ++ks) { bx[ks] = (32 * tb + r32) * 128 + 16 * (((2 * ks) | hh) ^ ((r32 >> 1) & 7)); asm volatile("" : "+v"(bx[ks])); }
    #pragma unroll
    for (int b = 0; b < 2; ++b) { bw[b] = W_RING + b * 3 * WSLOT + gm * W_M + (8 * hh + q4) * 192 + 96 * ug + 16 * (p4 >> 1) + 8 * (p4 & 1); asm volatile("" : "+v"(bw[b])); }
    auto rdx = [&](int kg) { return lds_ld<s16x8>(bx[kg & 3] + ((kg >> 2) & 3) * XSLOT); };
    auto rdw = [&](int kg, int uu) { const int s = (kg >> 1) % 6; const unsigned a = bw[s / 3] + (s % 3) * WSLOT + (kg & 1) * 3072 + 32 * uu; return lds_tr2(a, a + 768); };
    const bf16x2 ones = {(__bf16)1.0f, (__bf16)1.0f};
    const unsigned vo_cs = WS_CS + ((ng * 6 + 3 * ug + tb) * 2 + ((mp >> 2) & 1)) * 1024 + ((mp >> 3) * 32 + r32) * 16 + (mp & 3) * 4;
    auto csum = [&](const s16x8 (&a0)[NU], const s16x8 (&a1)[NU]) {
        if (tb < NU) {
            float c0 = 0.f, c1 = 0.f;
            #pragma unroll
            for (int uu = 0; uu < NU; ++uu)
                if (tb == uu) {
                    #pragma unroll
                    for (int i = 0; i < 4; ++i) {
                        const unsigned t0 = __builtin_bit_cast(u32x4, a0[uu])[i], t1 = __builtin_bit_cast(u32x4, a1[uu])[i];
                        c0 = __builtin_amdgcn_fdot2_f32_bf16(__builtin_bit_cast(bf16x2, t0), ones, c0, false);
                        c1 = __builtin_amdgcn_fdot2_f32_bf16(__builtin_bit_cast(bf16x2, t1), ones, c1, false);
                    }
                }
            c0 += c1;
            const auto sw = __builtin_amdgcn_permlane32_swap(__float_as_uint(c0), __float_as_uint(c0), false, false);
            const unsigned s0 = sw[0], s1 = sw[1];
            c0 = __uint_as_float(s0) + __uint_as_float(s1);
            const unsigned hi = f2bf(c0), lo = f2bf(c0 - bf2f(hi));
            if (hh == 0) __builtin_amdgcn_raw_buffer_store_b32(hi | (lo << 16), rs_ws, vo_cs, 0, AUX_SC1);
        }
    };
    unsigned vo_fx = WS_FLAGX + 4 * (32 * v + r32), vo_fc = WS_FLAGC + ((ng * 6 + 3 * ug) * 16 + (lane < 16 * NU ? lane : (lane & 15))) * 4;
    unsigned vo_fp = WS_FLAGC + ((ng * 6 + 3 * ug + tb) * 16 + mp) * 4, vo_st = stat_off, vo_l16 = lane * 16, vtag = tag;
    asm volatile("" : "+v"(vo_fx), "+v"(vo_fc), "+v"(vo_fp), "+v"(vo_st), "+v"(vo_l16), "+v"(vtag));
    s16x8 A[3][NU], B[3];
    #pragma unroll
    for (int k0 = 0; k0 < 2; ++k0) {
        #pragma unroll
        for (int uu = 0; uu < NU; ++uu) A[k0][uu] = rdw(k0, uu);
        B[k0] = rdx(k0);
    }
    TICK_DECL;
    TICK(0);
    if (mp < 8) csum(A[0], A[1]);
    unsigned fx = 0, fc = 0;
    auto late_load = [&](int i) {
        if (i < 2 * NU) {
            const unsigned so = WS_CS + ((ng * 6 + 3 * ug + (i >> 1)) * 2 + (i & 1)) * 1024;
            asm volatile("buffer_load_dwordx4 %0, %1, %2, %3 offen sc1" : "=v"(csf[i >> 1][i & 1]) : "v"(vo_l16), "s"(rs_ws), "s"(so) : "memory");
        }
        if (i == 2 * NU) asm volatile("buffer_load_dwordx2 %0, %1, %2, 0 offen sc1" : "=v"(gran) : "v"(vo_st), "s"(rs_ws) : "memory");
    };
    #pragma unroll
    for (int S = 0; S < 16; ++S) {
        if (S == 5 && tb < NU) {
            WAITV(0);
            asm volatile("s_mov_b64 exec, 1\n\tbuffer_store_dword %0, %1, %2, 0 offen sc1\n\ts_mov_b64 exec, -1" :: "v"(vtag), "v"(vo_fp), "s"(rs_ws) : "memory");
        }
        if (S == 13) {
            WAITV(2 * NU + 1); asm volatile("" : "+v"(fx));
            if (!all_eq_hp(fx, tag))
                for (unsigned spins = 0; !spin_fail(spins, ws, tag);) { fx = ld_sc1_u32(ws + vo_fx); if (__all(fx == tag)) break; }
        }
        if (S >= 1 && S < 14) BAR();
        TICK(1 + S);
        if (S == 10) asm volatile("buffer_load_dword %0, %1, %2, 0 offen sc1" : "=v"(fc) : "v"(vo_fc), "s"(rs_ws) : "memory");
        if (S == 11) asm volatile("buffer_load_dword %0, %1, %2, 0 offen sc1" : "=v"(fx) : "v"(vo_fx), "s"(rs_ws) : "memory");
        if (S == 12) {
            WAITV(1); asm volatile("" : "+v"(fc));
            if (!all_eq_hp(fc, tag))
                for (unsigned spins = 0; !spin_fail(spins, ws, tag);) { fc = ld_sc1_u32(ws + vo_fc); if (__all(fc == tag)) break; }
        }
        #pragma unroll
        for (int ks = 0; ks < 2; ++ks) {
            const int kg = 2 * S + ks, c = kg % 3, nx = (kg + 2) % 3;
            acc[0] = MFMA_BF(A[c][0], B[c], acc[0]);
            SB0();
            if (kg + 2 < 32) { A[nx][0] = rdw(kg + 2, 0); B[nx] = rdx(kg + 2); }
            if (S == 12) late_load(NU == 3 ? 3 * ks : ks);
            SB0();
            if constexpr (NU == 3) {
                acc[1] = MFMA_BF(A[c][1], B[c], acc[1]);
                SB0();
                if (kg + 2 < 32) A[nx][1] = rdw(kg + 2, 1);
                if (S == 12) late_load(3 * ks + 1);
                SB0();
                acc[2] = MFMA_BF(A[c][2], B[c], acc[2]);
                SB0();
                if (kg + 2 < 32) A[nx][2] = rdw(kg + 2, 2);
                if (S == 12) late_load(3 * ks + 2);
            }
            SB0();
        }
        if (S == 12) late_load(2 * NU);
        if (S == 0 && mp >= 8) csum(A[2], A[0]);
        SB0();
    }
    TICK(17);
    if (v == 0) TICK_DUMP(18, 0);
}

constexpr int XN_LOADS = 5;
struct G1Sched { int nw[14]; };
constexpr G1Sched g1_sched() {
    G1Sched r{};
    int wend[16] = {7, 10, 17, 20, 27, 30}, xend[8] = {4, 14, 24, 34}, cum = 36;
    {
        int c = 36;
        for (int S = 1; S < 14; ++S) {
            if (S >= 2 && S <= 9) { c += 2; xend[(S + 6) >> 1] = c; }
            if (S >= 1 && S <= 10) { c += 3; wend[S + 5] = c; }
        }
    }
    for (int S = 1; S < 14; ++S) {
        const int st = S == 13 ? 15 : S + 1;
        const int need = wend[st] > xend[st >> 1] ? wend[st] : xend[st >> 1];
        r.nw[S] = cum - need;
        if (S >= 2 && S <= 9) cum += 2;
        if (S >= 1 && S <= 10) cum += 3;
    }
    return r;
}

__global__ __launch_bounds__(768, 3) void fused_kernel(Params p) {
    extern __shared__ __attribute__((aligned(16))) unsigned char lds_raw[];
    const int tid = threadIdx.x, lane = tid & 63;
    const int v = __builtin_amdgcn_readfirstlane(tid >> 6);
    const int w = blockIdx.x, g = w & 7, j = w >> 3;
    const int pg = g & 1, gg = g >> 1;
    const int mp = 8 * pg + (j & 7), ng = gg + 4 * (j >> 3);
    const int U0 = ng < 11 ? 6 * ng : 66 + 4 * (ng - 11);
    const int nunits = ng < 11 ? 6 : 4;
    const int tb8 = 128 * pg + 32 * gg + j;
    const int d0 = 64 * g + 2 * j;
    const int rt = 4 * g + (j >> 3), ct = j & 7;
    unsigned char* ws = p.ws;
    const rsrc_t rs_ws = mk_rsrc(ws, WS_END);
    const int r32 = lane & 31, hh = lane >> 5;
    const int G = lane >> 4, q4 = (lane >> 2) & 3, p4 = lane & 3, gm = G & 1;
    STAMP(0); STAMPC(30);

    if (v >= 8) {
        const int hv = v - 8;
        const int ch = hv >> 1, nh = hv & 1, d = d0 + ch;
        unsigned pa, pi, pcr0, pci0, pcr1, pci1, pdt, dskb;
        {
            const unsigned o0 = (d * NS_ + 64 * nh + lane) * 2, o1 = (d * NS_ + 64 * (nh ^ 1) + lane) * 2, od = d * 2;
            asm volatile("global_load_ushort %0, %1, %2" : "=v"(pa) : "v"(o0), "s"(p.logA) : "memory");
            asm volatile("global_load_ushort %0, %1, %2" : "=v"(pi) : "v"(o0), "s"(p.Aim) : "memory");
            asm volatile("global_load_ushort %0, %1, %2" : "=v"(pcr0) : "v"(o0), "s"(p.Cre) : "memory");
            asm volatile("global_load_ushort %0, %1, %2" : "=v"(pci0) : "v"(o0), "s"(p.Cim) : "memory");
            asm volatile("global_load_ushort %0, %1, %2" : "=v"(pcr1) : "v"(o1), "s"(p.Cre) : "memory");
            asm volatile("global_load_ushort %0, %1, %2" : "=v"(pci1) : "v"(o1), "s"(p.Cim) : "memory");
            asm volatile("global_load_ushort %0, %1, %2" : "=v"(pdt) : "v"(od), "s"(p.logdt) : "memory");
            asm volatile("global_load_ushort %0, %1, %2" : "=v"(dskb) : "v"(od), "s"(p.Dskip) : "memory");
        }
        SB0();
        __builtin_amdgcn_s_setprio(2);
        const int a = hv & 1, hb = hv >> 1;
        const rsrc_t rs_x = mk_rsrc(p.x, L_ * D_ * 2);
        const rsrc_t rs_w = mk_rsrc(a ? p.wu : p.wg, D_ * F_ * 2);
        unsigned vo_x[2], vo_w[3];
        #pragma unroll
        for (int e = 0; e < 2; ++e) vo_x[e] = (lane >> 3) * 1024 + 16 * ((lane & 7) ^ ((4 * e + (lane >> 4)) & 7));
        #pragma unroll
        for (int i = 0; i < 3; ++i) { const int c = 64 * i + lane, row = c / 12, col = c - 12 * row; vo_w[i] = row * (F_ * 2) + col * 16; }
        const int rot = ROT;
        auto xhalf = [&](int xa, int hf) {
            const unsigned ko = 128 * ((xa + (rot >> 1)) & 7);
            #pragma unroll
            for (int i = 0; i < 2; ++i)
                dma16((xa & 3) * XSLOT + (8 * hf + 4 * a + 2 * hb + i) * 1024, vo_x[i], rs_x, (unsigned)((128 * mp + 64 * hf + 32 * a + 16 * hb + 8 * i) * 1024) + ko);
        };
        auto wdma = [&](int S, int i) { dma16(W_RING + (S % 6) * WSLOT + a * W_M + (3 * hb + i) * 1024, vo_w[i], rs_w, (unsigned)((32 * ((S + rot) & 15) + 16 * hb) * (F_ * 2) + 32 * U0)); };
        auto wst = [&](int S) { wdma(S, 0); wdma(S, 1); wdma(S, 2); };
        xhalf(0, 0); xhalf(0, 1); wst(0); wst(1); xhalf(1, 0); xhalf(1, 1); wst(2); wst(3); xhalf(2, 0); xhalf(2, 1); wst(4); wst(5); xhalf(3, 0); xhalf(3, 1);
        WAITV(24);
        if (tid == 512) lds_st<u32x4>(L_CNT, u32x4{0u, 0u, 0u, 0u});
        BARL();
        asm volatile("" : "+v"(pa), "+v"(pi), "+v"(pcr0), "+v"(pci0), "+v"(pcr1), "+v"(pci1), "+v"(pdt), "+v"(dskb));
        STAMP(4);
        #pragma unroll
        for (int r = 0; r < 2; ++r) {
            const int cb = 128 * hv + 64 * r;
            const unsigned ra = L_LN + q4 * 1024 + (cb + 16 * (lane >> 4) + 4 * p4) * 2;
            const s16x8 o = lds_tr2(ra, ra + 4096);
            __builtin_amdgcn_raw_buffer_store_b128(__builtin_bit_cast(u32x4, o), rs_ws, WS_XNT + ((cb + lane) >> 3) * 32768 + tb8 * 128 + (lane & 7) * 16, 0, AUX_SC1);
        }
        const unsigned tag = __builtin_amdgcn_readfirstlane(lds_ld<unsigned>(L_SCL + 8));
        const unsigned img = L_KG + hv * KGW;
        const int fq = lane >> 4, fr = lane & 15;
        unsigned fa = img + fr * 272 + fq * 16;
        unsigned pt0 = img + 4 * (16 * fr + 4 * fq), pt1 = img + 4 * (fq < 2 ? 256 + 8 * fr + 4 * fq : (fq == 2 ? 384 : 448) + 4 * fr);
        unsigned pt2 = img + 4 * (fq < 2 ? 512 + 64 * fq + 2 * fr : ((fq == 2 && fr == 0) ? 640 : 644)), pt3 = fq < 2 ? pt2 + 128 : img + 4 * 644;
        asm volatile("" : "+v"(fa), "+v"(pt0), "+v"(pt1), "+v"(pt2), "+v"(pt3));
        C2 lo16[16], hi16[16], hj16[16], hk[9], m16, m1, m2, m4, m8, m256, b1024, b1280;
        unsigned PL[16], P0[16], P1[16], P2[9];
        float isc_out = 0.f, k_dt = 0.f, k_mx = 0.f, k_skip = 0.f;
        auto pack4 = [&](unsigned* P, const C2* z, int r0) {
            #pragma unroll
            for (int i = r0; i < r0 + 4; ++i) { P[i] = pk_f16(z[i].x, z[i].y); asm volatile("" : "+v"(P[i])); }
        };
        auto kslot = [&](int n) {
            SB0();
            if (n == 0) { k_dt = rbf(__expf(bf2f(pdt))); m16.x = -rbf(__expf(bf2f(pa))); asm volatile("" : "+v"(m16), "+v"(k_dt)); }
            if (n == 1) {
                m16 = C2{m16.x * k_dt * 1.4426950408889634f, bf2f(pi) * k_dt * 0.15915494309189535f};
                hi16[0] = C2{bf2f(pcr0) * k_dt, bf2f(pci0) * k_dt};
                asm volatile("" : "+v"(m16), "+v"(hi16[0]));
            }
            if (n == 2) {
                k_mx = fmaxf(fmaxf(fmaxf(fabsf(hi16[0].x), fabsf(hi16[0].y)), fmaxf(fabsf(bf2f(pcr1) * k_dt), fabsf(bf2f(pci1) * k_dt))), fabsf(bf2f(dskb)) * (1.f / 128.f));
                asm volatile("" : "+v"(k_mx));
            }
            if (n == 3) { k_mx = wave_max(k_mx); asm volatile("" : "+v"(k_mx)); }
            if (n == 4) {
                int e = (int)((__float_as_uint(k_mx) >> 23) & 0xFF);
                e = e < 10 ? 10 : (e > 240 ? 240 : e);
                const float scale2 = __uint_as_float((unsigned)(260 - e) << 23);
                isc_out = __uint_as_float((unsigned)(e - 5) << 23);
                hi16[0] = C2{hi16[0].x * scale2, -hi16[0].y * scale2};
                k_skip = (nh == 0 && lane == 0) ? bf2f(dskb) * (0.5f * scale2) : 0.f;
                asm volatile("" : "+v"(hi16[0]), "+v"(isc_out), "+v"(k_skip));
            }
            if (n == 5) m1 = cexp_p(m16.x, -m16.y, 1.f);
            if (n == 6) m256 = cexp_p(m16.x, -m16.y, 256.f);
            if (n == 7) { m16 = cexp_p(m16.x, m16.y, 16.f); lo16[0] = C2{1.f, 0.f}; lo16[1] = m16; }
            if (n >= 8 && n <= 22) {
                const int i = n - 7;
                if (i <= 14) cmul2(lo16[i + 1], lo16[i], m16, hi16[i], hi16[i - 1], m1);
                else cmul2(hj16[0], hi16[0], m256, hi16[15], hi16[14], m1);
            }
            if (n == 23) cmul2(m2, m1, m1, hj16[8], hj16[0], m256);
            if (n == 24) cmul2(m4, m2, m2, hj16[12], hj16[8], m256);
            if (n == 25) cmul2(m8, m4, m4, b1024, hj16[12], m256);
            if (n >= 26 && n <= 29) { pack4(PL, lo16, 4 * (n - 26)); pack4(P0, hi16, 4 * (n - 26)); }
            if (n >= 30 && n <= 32) cmul2(hj16[n - 29], hj16[n - 30], m2, hj16[n - 21], hj16[n - 22], m4);
            if (n >= 33 && n <= 35) cmul2(hj16[n - 29], hj16[n - 30], m2, hj16[n - 20], hj16[n - 21], m4);
            if (n == 36) { cmul2(hj16[7], hj16[6], m2, b1280, b1024, m256); hk[0] = b1024; hk[2] = b1280; }
            if (n == 37) cmul2(hk[4], b1280, m256, hk[1], b1024, m8);
            if (n == 38) cmul2(hk[6], hk[4], m256, hk[3], b1280, m8);
            if (n == 39) cmul2(hk[8], hk[6], m256, hk[5], hk[4], m8);
            if (n == 40) { C2 dm; cmul2(hk[7], hk[6], m8, dm, hk[6], m8); }
            if (n == 41 || n == 42) { pack4(P1, hj16, 8 * (n - 41)); pack4(P1, hj16, 8 * (n - 41) + 4); }
            if (n == 43) { pack4(P2, hk, 0); pack4(P2, hk, 4); P2[8] = pk_f16(hk[8].x, hk[8].y); asm volatile("" : "+v"(P2[8])); }
            SB0();
        };
        constexpr G1Sched sc = g1_sched();
        u32x4 rc[2], rn[2], re;
        #pragma unroll
        for (int S = 1; S <= 10; ++S) {
            WAITV(sc.nw[S]);
            BAR();
            const int n0 = S == 1 ? 0 : 3 + 4 * (S - 2);
            if (S >= 2 && S <= 9) { xhalf((S + 6) >> 1, (S + 6) & 1); kslot(n0); }
            #pragma unroll
            for (int i = 0; i < 3; ++i) { wdma(S + 5, i); kslot(n0 + (S >= 2 && S <= 9 ? 1 : 0) + i); }
            if (S == 10) kslot(38);
            if (S == 5 && tid == 512) st_sc1_u32(ws + WS_FLAGX + 4 * tb8, tag);
        }
        STAMP(5);
        WAITV(sc.nw[11]);
        BAR();
        kslot(39); kslot(40); kslot(41); kslot(42);
        WAITV(sc.nw[12]);
        BAR();
        kslot(43);
        WAITV(sc.nw[13]);
        BAR();
        {
            const unsigned xb = WS_XNT + (d0 >> 3) * 32768 + ((d0 & 7) + hh) * 16;
            #pragma unroll
            for (int i = 0; i < 2; ++i) {
                const int tbk = 64 * hv + 32 * i + r32;
                const unsigned o0 = xb + tbk * 128, o1 = tbk < 255 ? o0 + 128 : 0x7FFFFFF0u;
                asm volatile("buffer_load_dwordx4 %0, %1, %2, 0 offen sc1" : "=v"(rc[i]) : "v"(o0), "s"(rs_ws) : "memory");
                asm volatile("buffer_load_dwordx4 %0, %1, %2, 0 offen sc1" : "=v"(rn[i]) : "v"(o1), "s"(rs_ws) : "memory");
            }
            const unsigned oe = r32 == 3 ? xb : 0x7FFFFFF0u;
            asm volatile("buffer_load_dwordx4 %0, %1, %2, 0 offen sc1" : "=v"(re) : "v"(oe), "s"(rs_ws) : "memory");
            if (w == 0 && tid == 512) st_sc1_u32(ws + WS_EPOCH, tag);
        }
        TICK_DECL2; TICK2(0);
        WAITV(0);
        #pragma unroll
        for (int i = 0; i < 2; ++i) asm volatile("" : "+v"(rc[i]), "+v"(rn[i]));
        asm volatile("" : "+v"(re));
        u32x4 us0[2], us1[2], us2[2], us3[2];
        #pragma unroll
        for (int i = 0; i < 2; ++i) {
            const u32x4 c = rc[i], n = rn[i];
            us0[i] = u32x4{__builtin_amdgcn_alignbyte(c[1], c[0], 2), __builtin_amdgcn_alignbyte(c[2], c[1], 2), __builtin_amdgcn_alignbyte(c[3], c[2], 2), __builtin_amdgcn_alignbyte(n[0], c[3], 2)};
            us1[i] = u32x4{c[1], c[2], c[3], n[0]};
            us2[i] = u32x4{us0[i][1], us0[i][2], us0[i][3], __builtin_amdgcn_alignbyte(n[1], n[0], 2)};
            us3[i] = u32x4{c[2], c[3], n[0], n[1]};
            asm volatile("" : "+v"(us0[i]), "+v"(us1[i]), "+v"(us2[i]), "+v"(us3[i]));
        }
        u32x4 ue0 = {0u, 0u, 0u, re[0] << 16}, ue1 = {0u, 0u, 0u, re[0]}, ue2 = {0u, 0u, re[0] << 16, __builtin_amdgcn_alignbyte(re[1], re[0], 2)}, ue3 = {0u, 0u, re[0], re[1]};
        asm volatile("" : "+v"(ue0), "+v"(ue1), "+v"(ue2), "+v"(ue3));
        unsigned ub = L_U + hh * 4 * UCP + 16 * (4 + 64 * hv + r32), ub3 = (hh ? L_ULAST : L_U + 3 * UCP) + 16 * (4 + 64 * hv + r32);
        const int aq = 255 - (64 * hv + lane);
        unsigned kra = L_KR + (lane & 3) * KRS + 16 * (lane >> 2) + 256 * hv;
        unsigned kia = L_KG + 4 * (hv < 2 ? 384 + aq : (hv == 2 ? 256 + 2 * aq : (aq >= 32 ? 128 + 4 * aq : 8 * aq)));
        asm volatile("" : "+v"(ub), "+v"(ub3), "+v"(kra), "+v"(kia));
        TICK2(1);
        lds_wait_ge(C_M, 8);
        TICK2(2);
        {
#define PUT8(P, r0) asm volatile("s_mov_b32 m0, %8\n\ts_nop 0\n\t" \
                "ds_write_addtid_b32 %0 offset:%9\n\tds_write_addtid_b32 %1 offset:%9+272\n\tds_write_addtid_b32 %2 offset:%9+544\n\tds_write_addtid_b32 %3 offset:%9+816\n\t" \
                "ds_write_addtid_b32 %4 offset:%9+1088\n\tds_write_addtid_b32 %5 offset:%9+1360\n\tds_write_addtid_b32 %6 offset:%9+1632\n\tds_write_addtid_b32 %7 offset:%9+1904" \
                :: "v"(P[r0]), "v"(P[r0 + 1]), "v"(P[r0 + 2]), "v"(P[r0 + 3]), "v"(P[r0 + 4]), "v"(P[r0 + 5]), "v"(P[r0 + 6]), "v"(P[r0 + 7]), "s"(img - 61440), "i"(61440 + (r0) * 272) : "memory")
            auto frag = [&](int ks) { return __builtin_bit_cast(f16x8, lds_ld<s16x8>(fa + 64 * ks)); };
            f16x8 lo[4], f0[4], f1[4], f2[4];
            PUT8(PL, 0); PUT8(PL, 8);
            TICK3(15);
            #pragma unroll
            for (int ks = 0; ks < 4; ++ks) lo[ks] = frag(ks);
            SB0();
            PUT8(P0, 0); PUT8(P0, 8);
            #pragma unroll
            for (int ks = 0; ks < 4; ++ks) f0[ks] = frag(ks);
            SB0();
            PUT8(P1, 0); PUT8(P1, 8);
            #pragma unroll
            for (int ks = 0; ks < 4; ++ks) f1[ks] = frag(ks);
            SB0();
            PUT8(P2, 0);
            asm volatile("s_mov_b32 m0, %1\n\ts_nop 0\n\tds_write_addtid_b32 %0 offset:%2" :: "v"(P2[8]), "s"(img - 61440), "i"(61440 + 8 * 272) : "memory");
            #pragma unroll
            for (int ks = 0; ks < 4; ++ks) f2[ks] = frag(ks);
            TICK3(16);
            SB0();
            #pragma unroll
            for (int i = 0; i < 2; ++i) {
                lds_st<u32x4>(ub + 512 * i, us0[i]);
                lds_st<u32x4>(ub + 512 * i + UCP, us1[i]);
                lds_st<u32x4>(ub + 512 * i + 2 * UCP, us2[i]);
                lds_st<u32x4>(ub3 + 512 * i, us3[i]);
            }
            if (hv == 0 && r32 < 4) {
                lds_st<u32x4>(ub - 64, ue0); lds_st<u32x4>(ub - 64 + UCP, ue1); lds_st<u32x4>(ub - 64 + 2 * UCP, ue2); lds_st<u32x4>(ub3 - 64, ue3);
            }
            TICK3(17);
            SB0();
            f32x4 k0 = {0.f, 0.f, 0.f, 0.f}, k1 = k0, k2 = k0;
            #pragma unroll
            for (int ks = 0; ks < 4; ++ks) k0 = __builtin_amdgcn_mfma_f32_16x16x32_f16(f0[ks], lo[ks], k0, 0, 0, 0);
            #pragma unroll
            for (int ks = 0; ks < 4; ++ks) k1 = __builtin_amdgcn_mfma_f32_16x16x32_f16(f1[ks], lo[ks], k1, 0, 0, 0);
            #pragma unroll
            for (int ks = 0; ks < 4; ++ks) k2 = __builtin_amdgcn_mfma_f32_16x16x32_f16(f2[ks], lo[ks], k2, 0, 0, 0);
            k0[0] += k_skip;
            lds_st<f32x4>(pt0, k0); lds_st<f32x4>(pt1, k1);
            lds_st<f32x2>(pt2, f32x2{k2[0], k2[1]}); lds_st<f32x2>(pt3, f32x2{k2[2], k2[3]});
        }
        STAMP(6);
        if (nh == 0 && lane == 0) lds_st<float>(L_SCL + 4 * ch, isc_out);
        TICK2(3);
        grp_bar(C_H, 4, lane);
        TICK2(4); TICK2(5);
        STAMP(7);
        #pragma unroll
        for (int c2 = 0; c2 < 2; ++c2) {
            const unsigned ka = kia + c2 * 2 * KGW, kr = kra + c2 * KRB;
            auto k1 = [&](int o) { return lds_ld<float>(ka + 4 * o) + lds_ld<float>(ka + KGW + 4 * o); };
            auto k4 = [&](int o) { return lds_ld<f32x4>(ka + 4 * o) + lds_ld<f32x4>(ka + KGW + 4 * o); };
            u32x4 o;
            if (hv < 2) {
                const unsigned ab = pk_f16(k1(0), k1(1));
                const unsigned AA = __builtin_amdgcn_perm(ab, ab, 0x01000100u), BB = __builtin_amdgcn_perm(ab, ab, 0x03020302u), BA = __builtin_amdgcn_perm(ab, ab, 0x01000302u);
                o = u32x4{BB, (aq & 1) ? BB : BA, AA, AA};
            } else if (hv == 2) {
                const float A = k1(0), B = k1(1), C = k1(2);
                o = u32x4{pk_f16(C, C), pk_f16(B, B), pk_f16(B, A), pk_f16(A, A)};
            } else if (aq >= 32) {
                const f32x4 t = k4(0);
                const float E = k1(4);
                o = u32x4{pk_f16(E, t[3]), pk_f16(t[2], t[2]), pk_f16(t[2], t[1]), pk_f16(t[0], t[0])};
            } else {
                const f32x4 t0 = k4(0), t1 = k4(4);
                o = u32x4{pk_f16(t1[3], t1[2]), pk_f16(t1[1], t1[0]), pk_f16(t0[3], t0[2]), pk_f16(t0[1], t0[0])};
            }
            lds_st<u32x4>(kr, o);
            if (hv < 2 && (hv == 0 || lane < 60)) lds_st<u32x4>(kr + 1024, u32x4{0u, 0u, 0u, 0u});
        }
        STAMP(8);
        TICK2(6);
        grp_bar(C_H, 8, lane);
        TICK2(7);
        const int half = nh;
        const float isc = lds_ld<float>(L_SCL + 4 * ch);
        __builtin_amdgcn_s_setprio(0);
        lds_wait_ge(C_GELU, 8);
        STAMP(9); STAMPC(25);
        TICK2(8);
        {
            f32x16 y0 = {}, y1 = {};
            unsigned a_u = ((ch && q4 == 3) ? L_ULAST : L_U + ch * 4 * UCP + q4 * UCP) + 2 * (8 * hh + 16 * gm + 4 * p4) + 1024 * half;
            unsigned a_k = L_KR + ch * KRB + hh * KRS + 16 * (63 - r32 + 16 * half) - 512;
            asm volatile("" : "+v"(a_u), "+v"(a_k));
            auto rdk = [&](int i, int far) { return lds_ld<s16x8>(a_k + (far ? 0 : 512) + (i & 1) * 2 * KRS + 16 * (i >> 1)); };
            auto rdu = [&](int i, int far) { return lds_tr2(a_u + 32 * i + (far ? 2048 : 0), a_u + 32 * i + (far ? 2056 : 8)); };
            s16x8 Ua[3], Ub[3], K0[3], K1[3];
            #pragma unroll
            for (int i = 0; i < 2; ++i) { Ua[i] = rdu(i, 0); K0[i] = rdk(i, 0); K1[i] = rdk(i, 1); Ub[i] = rdu(i, 1); }
            #pragma unroll
            for (int i = 0; i < 32; ++i) {
                const int c = i % 3, nx = (i + 2) % 3;
                y0 = MFMA_H(Ua[c], K0[c], y0);
                SB0();
                if (i + 2 < 32) { Ua[nx] = rdu(i + 2, 0); }
                SB0();
                y1 = MFMA_H(Ua[c], K1[c], y1);
                SB0();
                if (i + 2 < 32) { K0[nx] = rdk(i + 2, 0); K1[nx] = rdk(i + 2, 1); }
                SB0();
                y1 = MFMA_H(Ub[c], K0[c], y1);
                SB0();
                if (i + 2 < 32) { Ub[nx] = rdu(i + 2, 1); }
                SB0();
            }
            STAMP(10); STAMPC(26);
            TICK2(9);
            const unsigned rb = L_CRED + ch * CRED_W + r32 * 144 + hh * 16;
            #pragma unroll
            for (int gq = 0; gq < 4; ++gq) {
                if (half) lds_st<f32x4>(rb + gq * 32, f32x4{y0[4 * gq], y0[4 * gq + 1], y0[4 * gq + 2], y0[4 * gq + 3]});
                else lds_st<f32x4>(rb + 32 * 144 + gq * 32, f32x4{y1[4 * gq], y1[4 * gq + 1], y1[4 * gq + 2], y1[4 * gq + 3]});
            }
            TICK2(10);
            grp_bar(C_H, 12, lane);
            TICK2(11);
            const unsigned ob = rb + (half ? 32 * 144 : 0);
            const unsigned sg = L_KR + hv * 2560;
            #pragma unroll
            for (int gq = 0; gq < 4; ++gq) {
                const f32x4 o = lds_ld<f32x4>(ob + gq * 32);
                float y[4];
                #pragma unroll
                for (int i = 0; i < 4; ++i) y[i] = ((half ? y1[4 * gq + i] : y0[4 * gq + i]) + o[i]) * isc;
                lds_st<u32x2>(sg + r32 * 80 + gq * 16 + hh * 8, u32x2{pk_bf(y[0], y[1]), pk_bf(y[2], y[3])});
            }
            TICK2(12);
            #pragma unroll
            for (int i = 0; i < 2; ++i) {
                const u32x4 o = lds_ld<u32x4>(sg + (16 * i + (lane >> 2)) * 80 + (lane & 3) * 16);
                __builtin_amdgcn_raw_buffer_store_b128(o, rs_ws, WS_AT + (d0 + ch) * 4096 + half * 2048 + i * 1024 + lane * 16, 0, AUX_SC1);
            }
            TICK2(13);
            grp_bar(C_H, 16, lane);
        }
        STAMP(11);
        {
            const rsrc_t rs_wd = mk_rsrc(p.wd, D_ * F_ * 2);
            const unsigned vo_d = (lane >> 3) * 1024 + 16 * ((lane & 7) ^ (((lane >> 4) & 1) << 2));
            auto stage2 = [&](int st) {
                #pragma unroll
                for (int e = 0; e < 4; ++e) {
                    const int U = 8 * st + 2 * hv + (e >> 1);
                    dma16(G2S[st] + (2 * hv + (e >> 1)) * 2048 + (e & 1) * 1024, vo_d, rs_wd, (unsigned)((16 * U + 8 * (e & 1)) * 1024 + 128 * ct));
                }
            };
            __builtin_amdgcn_s_setprio(2);
            stage2(5); stage2(6); stage2(7); stage2(8);
            WAITV(16);
            if (lane == 0) st_sc1_u32(ws + WS_FLAGA + ((half * 256 + (d0 >> 1)) * 2 + ch) * 4, tag);
            TICK2(14);
            if (v == 8) TICK2_DUMP(18, 1);
            WAITV(13); BAR();
            stage2(9); stage2(10);
            WAITV(16); BAR();
            WAITV(12); BAR();
            WAITV(8); BAR();
            WAITV(4); BAR();
            WAITV(0); BAR();
        }
        STAMP(16); STAMPC(31);
        return;
    }

    const int tb = v & 3, ug = v >> 2;
    unsigned tag;
    {
        const u32x4 xrow = *(const GAS u32x4*)((const unsigned char*)p.x + (size_t)(8 * tb8 + v) * 1024 + lane * 16);
        const unsigned ep = ld_sc1_u32(ws + WS_EPOCH);
        f32x2 xv[4];
        #pragma unroll
        for (int i = 0; i < 4; ++i) xv[i] = f32x2{bf2f(xrow[i] & 0xFFFFu), __uint_as_float(xrow[i] & 0xFFFF0000u)};
        const f32x2 s2 = (xv[0] + xv[1]) + (xv[2] + xv[3]);
        const float mean = wave_sum(s2.x + s2.y) * (1.f / 512.f);
        f32x2 q2 = {0.f, 0.f};
        #pragma unroll
        for (int i = 0; i < 4; ++i) { const f32x2 dd = xv[i] - mean; q2 = dd * dd + q2; }
        const float var = wave_sum(q2.x + q2.y) * (1.f / 512.f);
        const float mb = rbf(mean), sb = rbf(__builtin_amdgcn_sqrtf(rbf(rbf(var) + 1e-5f)));
        const float ri = __builtin_amdgcn_rcpf(sb);
        u32x4 o;
        #pragma unroll
        for (int i = 0; i < 4; ++i) {
            const f32x2 dd = xv[i] - mb;
            const unsigned a = pk_bf(dd.x, dd.y);
            const f32x2 af = {bf2f(a & 0xFFFFu), __uint_as_float(a & 0xFFFF0000u)};
            f32x2 qq = af * ri;
            qq = (af - qq * sb) * ri + qq;
            const unsigned b = pk_bf(qq.x, qq.y);
            o[i] = pk_f16(bf2f(b & 0xFFFFu), __uint_as_float(b & 0xFFFF0000u));
        }
        lds_st<u32x4>(L_LN + v * 1024 + lane * 16, o);
        SB0();
        tag = __builtin_amdgcn_readfirstlane(ep) + 1u;
        if (tid == 0) lds_st<unsigned>(L_SCL + 8, tag);
        if (lane == 0) {
            const unsigned long long gr = ((unsigned long long)tag << 32) | (f2bf(mb) | (f2bf(sb) << 16));
            __hip_atomic_store((GAS unsigned long long*)(ws + WS_STATS + (8 * tb8 + v) * 8), gr, RLX_AGENT);
        }
        STAMP(1);
    }
    BARL();
    STAMP(4); STAMPC(20);

    f32x16 acc[3] = {};
    u32x4 csf[3][2];
    unsigned long long gran = 0;
    const bool full = ug == 0 || nunits == 6;
    const unsigned stat_off = WS_STATS + (128 * mp + 32 * tb + r32) * 8;
    if (full) gemm1_loop<3>(acc, csf, gran, v, tb, ug, mp, ng, lane, tag, ws, rs_ws, stat_off);
    else gemm1_loop<1>(acc, csf, gran, v, tb, ug, mp, ng, lane, tag, ws, rs_ws, stat_off);
    STAMP(5); STAMPC(21);
    TICK_DECL2; TICK2(0);
    lds_arrive(C_M);
    WAITV(0);
    asm volatile("" : "+v"(gran));
    #pragma unroll
    for (int uu = 0; uu < 3; ++uu) asm volatile("" : "+v"(csf[uu][0]), "+v"(csf[uu][1]));
    for (unsigned spins = 0; !__all((unsigned)(gran >> 32) == tag);) {
        if (spin_fail(spins, ws, tag)) break;
        gran = __hip_atomic_load((const GAS unsigned long long*)(ws + stat_off), RLX_AGENT);
    }
    TICK2(1);
    const float t_mean = bf2f((unsigned)gran & 0xFFFFu), t_s = __uint_as_float((unsigned)gran & 0xFFFF0000u), t_ri = __builtin_amdgcn_rcpf(t_s);
    const float t_s2 = t_s * t_s, gel_a = -0.10294324f * t_ri * t_ri * t_ri, gel_b = -2.3022082f * t_ri;
    const rsrc_t rs_wd = mk_rsrc(p.wd, D_ * F_ * 2);
    const unsigned vo_d = (lane >> 3) * 1024 + 16 * ((lane & 7) ^ (((lane >> 4) & 1) << 2));
    auto wd_dma = [&](int i) {
        const int st = i >> 1, U = 8 * st + v;
        dma16(G2S[st] + v * 2048 + (i & 1) * 1024, vo_d, rs_wd, (unsigned)((16 * U + 8 * (i & 1)) * 1024 + 128 * ct));
    };
    {
        const unsigned nm = pk_bf(-t_mean, -t_mean);
        const s16x8 Bm = __builtin_bit_cast(s16x8, u32x4{nm, nm, nm, nm});
        #pragma unroll
        for (int uu = 0; uu < 3; ++uu)
            if (uu == 0 || full) {
                acc[uu] = MFMA_BF(__builtin_bit_cast(s16x8, csf[uu][0]), Bm, acc[uu]);
                acc[uu] = MFMA_BF(__builtin_bit_cast(s16x8, csf[uu][1]), Bm, acc[uu]);
            }
        TICK2(2);
        TICK2(3);
        #pragma unroll
        for (int uu = 0; uu < 3; ++uu) {
            u32x4 o;
            #pragma unroll
            for (int i = 0; i < 4; ++i) {
                if (uu == 0 || full) {
                    const f32x2 ga = f32x2{acc[uu][2 * i], acc[uu][2 * i + 1]}, ua = f32x2{acc[uu][8 + 2 * i], acc[uu][9 + 2 * i]};
                    const f32x2 zz = ga * (ga * ga * gel_a + gel_b);
                    const f32x2 den = f32x2{__builtin_amdgcn_exp2f(zz.x), __builtin_amdgcn_exp2f(zz.y)} * t_s2 + t_s2;
                    const f32x2 hv = ga * ua * f32x2{__builtin_amdgcn_rcpf(den.x), __builtin_amdgcn_rcpf(den.y)};
                    o[i] = pk_bf(hv.x, hv.y);
                }
            }
            if (uu == 0 || full) __builtin_amdgcn_raw_buffer_store_b128(o, rs_ws, WS_H + ((4 * mp + tb) * 86 + U0 + 3 * ug + uu) * 1024 + lane * 16, 0, AUX_SC1);
            TICK2(4 + uu);
        }
    }
    lds_arrive(C_GELU);
    STAMP(6); STAMPC(22);

    {
        const int th = v & 1, kq = v >> 1;
        const int tk = tid >> 3, ck = tid & 7;
        WAITV(0);
        TICK2(7);
        grp_bar(C_M, 16, lane);
        TICK2(8);
        if (tid == 0) st_sc1_u32(ws + WS_FLAGH + mp * 64 + 4 * ng, tag);
        STAMP(7);
        lds_wait_ge(C_H, 8);
        #pragma unroll
        for (int i = 0; i < 10; ++i) wd_dma(i);
        const u32x4 xv = *(const GAS u32x4*)((const unsigned char*)p.x + (size_t)(64 * rt + tk) * 1024 + (64 * ct + 8 * ck) * 2);
        if (v == 0) { wait_flags(ws, WS_FLAGH + (rt >> 1) * 64, 16, tag, lane); lds_arrive(C_HF); }
        WAITV(0);
        lds_arrive(C_HF);
        lds_wait_ge(C_HF, 9);
        STAMP(12); STAMPC(23);
        TICK2(9);
        const unsigned hbase = WS_H + (2 * rt + th) * 86 * 1024 + kq * 1024;
        u32x4 hf[22];
        const unsigned vl16 = lane * 16;
        auto ldh = [&](int i) {
            const int U = 8 * (i >> 1) + 4 * (i & 1);
            const unsigned so = (U + 3 < 86 || kq < 2) ? hbase + U * 1024 : hbase;
            asm volatile("buffer_load_dwordx4 %0, %1, %2, %3 offen sc1" : "=v"(hf[i]) : "v"(vl16), "s"(rs_ws), "s"(so) : "memory");
        };
        constexpr int HD = 12;
        #pragma unroll
        for (int i = 0; i < HD; ++i) ldh(i);
        f32x16 d0a = {}, d1a = {};
        const unsigned sw = ((q4 >> 1) & 1) << 2;
        unsigned bd[3][2];
        #pragma unroll
        for (int b = 0; b < 3; ++b)
            #pragma unroll
            for (int nb = 0; nb < 2; ++nb) { bd[b][nb] = kq * 2048 + (4 * hh + q4) * 128 + 16 * ((4 * nb + 2 * gm + (p4 >> 1)) ^ sw) + 8 * (p4 & 1) + b * 65536; asm volatile("" : "+v"(bd[b][nb])); }
        auto rdd = [&](int i, int nb) { const unsigned s = G2S[i >> 1] + (i & 1) * 8192; const unsigned a = bd[s >> 16][nb] + (s & 0xFFFFu); return lds_tr2(a, a + 1024); };
        s16x8 W0 = rdd(0, 0), W1 = rdd(0, 1);
        u32x4 at = {0u, 0u, 0u, 0u};
        #pragma unroll
        for (int st = 0; st < 11; ++st) {
            SB0();
            if (st == 4) TICK2(11);
            if (st == 7 && v == 0) wait_flags(ws, WS_FLAGA + ((rt >> 4) * 256 + 32 * ct) * 8, 64, tag, lane);
            if (st >= 4 && st < 10) BAR();
            if (st == 0) TICK2(10); if (st == 4) TICK2(12); if (st == 8) TICK2(13);
            if (st == 7) at = __builtin_amdgcn_raw_buffer_load_b128(rs_ws, WS_AT + (64 * ct + (tid >> 3)) * 4096 + (64 * rt + 8 * (tid & 7)) * 2, 0, AUX_SC1);
            SB0();
            #pragma unroll
            for (int e = 0; e < 2; ++e) {
                const int i = 2 * st + e;
                if (i + HD < 22) ldh(i + HD);
                const int rem = (i + HD < 22) ? HD : 21 - i;
                asm volatile("s_waitcnt vmcnt(%1)" : "+v"(hf[i]) : "i"(rem) : "memory");
                const s16x8 H = __builtin_bit_cast(s16x8, hf[i]);
                s16x8 N0 = W0, N1 = W1;
                if (i < 21 || kq < 2) {
                    d0a = MFMA_BF(W0, H, d0a);
                    SB0();
                    if (i + 1 < 22) N0 = rdd(i + 1, 0);
                    SB0();
                    d1a = MFMA_BF(W1, H, d1a);
                    SB0();
                    if (i + 1 < 22) N1 = rdd(i + 1, 1);
                }
                W0 = N0; W1 = N1;
                SB0();
            }
        }
        STAMP(14); STAMPC(24);
        TICK2(14);
        BARL();
        const unsigned rb = L_RED + kq * RED_W + (32 * th + r32) * 272 + hh * 16;
        #pragma unroll
        for (int gq = 0; gq < 4; ++gq) {
            lds_st<f32x4>(rb + gq * 32, f32x4{d0a[4 * gq], d0a[4 * gq + 1], d0a[4 * gq + 2], d0a[4 * gq + 3]});
            lds_st<f32x4>(rb + 128 + gq * 32, f32x4{d1a[4 * gq], d1a[4 * gq + 1], d1a[4 * gq + 2], d1a[4 * gq + 3]});
        }
        #pragma unroll
        for (int i = 0; i < 4; ++i) lds_st<unsigned>(L_ATT + (tid >> 3) * 132 + (tid & 7) * 16 + 4 * i, at[i]);
        BARL();
        TICK2(15);
        STAMP(15);
        f32x4 s0 = {0.f, 0.f, 0.f, 0.f}, s1 = s0;
        #pragma unroll
        for (int q = 0; q < 4; ++q) {
            const unsigned a = L_RED + q * RED_W + tk * 272 + ck * 32;
            s0 += lds_ld<f32x4>(a); s1 += lds_ld<f32x4>(a + 16);
        }
        float o[8];
        #pragma unroll
        for (int i = 0; i < 8; ++i) {
            const float av = bf2f(lds_ld<u16>(L_ATT + (8 * ck + i) * 132 + tk * 2));
            const float xx = (i & 1) ? __uint_as_float(xv[i >> 1] & 0xFFFF0000u) : bf2f(xv[i >> 1] & 0xFFFFu);
            o[i] = rbf(xx + av) + rbf(i < 4 ? s0[i & 3] : s1[i & 3]);
        }
        const u32x4 ov = {pk_bf(o[0], o[1]), pk_bf(o[2], o[3]), pk_bf(o[4], o[5]), pk_bf(o[6], o[7])};
        __builtin_amdgcn_raw_buffer_store_b128(ov, mk_rsrc(p.out, L_ * D_ * 2), (64 * rt + tk) * 1024 + (64 * ct + 8 * ck) * 2, 0, AUX_OUT);
        TICK2(16);
        if (v == 0) TICK2_DUMP(17, 0);
    }
    STAMP(16); STAMPC(31);
    STAMP_DUMP();
}

extern "C" void kernel_launch(void* const* d_in, const int* in_sizes, int n_in, void* d_out, int out_size, void* d_ws, size_t ws_size, hipStream_t stream) {
    (void)in_sizes; (void)n_in; (void)out_size; (void)ws_size;
    Params p;
    p.x = (const u16*)d_in[0]; p.logA = (const u16*)d_in[1]; p.Aim = (const u16*)d_in[2]; p.Cre = (const u16*)d_in[3]; p.Cim = (const u16*)d_in[4];
    p.logdt = (const u16*)d_in[5]; p.Dskip = (const u16*)d_in[6]; p.wg = (const u16*)d_in[8]; p.wu = (const u16*)d_in[9]; p.wd = (const u16*)d_in[10];
    p.out = (u16*)d_out; p.ws = (unsigned char*)d_ws;
    (void)hipFuncSetAttribute((const void*)fused_kernel, hipFuncAttributeMaxDynamicSharedMemorySize, LDS_BYTES);
    fused_kernel<<<256, 768, LDS_BYTES, stream>>>(p);
}
```

```cpp
#include <hip/hip_runtime.h>
#include <stdint.h>

#define LAS __attribute__((address_space(3)))
#define GAS __attribute__((address_space(1)))
#define DI __device__ __forceinline__
typedef unsigned short u16;
typedef short s16x4 __attribute__((ext_vector_type(4)));
typedef short s16x8 __attribute__((ext_vector_type(8)));
typedef _Float16 f16x8 __attribute__((ext_vector_type(8)));
typedef _Float16 f16x2 __attribute__((ext_vector_type(2)));
typedef __bf16 bf16x2 __attribute__((ext_vector_type(2)));
typedef float f32x2 __attribute__((ext_vector_type(2)));
typedef float f32x4 __attribute__((ext_vector_type(4)));
typedef float f32x16 __attribute__((ext_vector_type(16)));
typedef unsigned u32x2 __attribute__((ext_vector_type(2)));
typedef unsigned u32x4 __attribute__((ext_vector_type(4)));
typedef unsigned u32x8 __attribute__((ext_vector_type(8)));
typedef unsigned u32x16 __attribute__((ext_vector_type(16)));
typedef __amdgpu_buffer_rsrc_t rsrc_t;

constexpr int L_ = 2048, D_ = 512, NS_ = 128, F_ = 1376;
constexpr unsigned WS_EPOCH = 0, WS_TMO = 256, WS_FLAGX = 1024, WS_FLAGC = 12288, WS_STATS = 20480;
constexpr unsigned WS_FLAGH = 36864, WS_FLAGA = WS_FLAGH + 16384;
constexpr unsigned WS_XNT = 65536, WS_H = WS_XNT + 2097152, WS_AT = WS_H + 64 * 86 * 1024, WS_CS = WS_AT + 2097152, WS_END = WS_CS + 16 * 6 * 2048;
constexpr unsigned WS_STAMP = 16u << 20;
constexpr unsigned XSLOT = 16384, W_RING = 65536, WSLOT = 12352, W_M = 6176, MISC = W_RING + 6 * WSLOT;
constexpr unsigned L_KP = MISC;
constexpr unsigned L_SCL = L_KP + 2592;
constexpr unsigned L_CNT = L_SCL + 16;
constexpr unsigned C_M = L_CNT, C_H = L_CNT + 4, C_GELU = L_CNT + 8, C_HF = L_CNT + 12;
constexpr unsigned L_LN = L_CNT + 64;
constexpr unsigned L_CSF = L_LN, L_STT = L_LN + 12288, L_PAR = L_LN + 13312;
constexpr unsigned KGW = 4352, L_KG = 98304;
constexpr unsigned L_STAMP = L_LN + 18432;
constexpr unsigned L_END = L_STAMP + 768;
static_assert(L_END <= 163840, "lds");
static_assert(WS_STATS + 2048 * 8 <= WS_FLAGH && WS_FLAGA + 8192 <= WS_XNT, "ws");
constexpr unsigned UCP = 4160, L_U = 0;
constexpr unsigned KRS = 1568, KRB = 4 * KRS, L_KR = W_RING + 4 * WSLOT + 1024;
constexpr unsigned L_ULAST = L_KR + 2 * KRB + 192;
constexpr unsigned CRED_W = 64 * 144, L_CRED = L_LN;
static_assert(L_KG + 4 * KGW <= L_KR && (L_KR & 255) == 0, "images");
static_assert(L_U + 7 * UCP <= 2 * XSLOT && L_ULAST + UCP <= MISC && ((L_U + 7 * UCP) & 255) == (L_ULAST & 255) && L_CRED + 2 * CRED_W <= L_STAMP, "conv lds");
constexpr unsigned G2R = 30720, G2W = 5 * 2048;
static_assert(G2R + 8 * G2W <= L_KR && L_U + 7 * UCP <= G2R, "g2 ring");
constexpr unsigned part_a(int q) { return G2R + q * G2W; }
constexpr unsigned part_b(int q) { return q < 5 ? q * 6144 : G2R + 8 * G2W + (q - 5) * 6144; }
constexpr unsigned PAK = 40960, PBK = 59520;
constexpr unsigned L_ATT = (L_LN + 127) & ~127u;
static_assert(5 * 6144 <= G2R && part_b(7) + 6144 <= 131072 && part_a(3) < 65536 && part_a(4) >= PAK && part_a(7) - PAK < 65536 && PAK + 39 * 256 < 65536, "g2 lds");
static_assert(part_b(4) < 65536 && part_b(5) >= PBK && part_b(7) - PBK < 65536 && PBK + 23 * 256 < 65536 && L_ATT + 64 * 132 + 16 <= L_STAMP, "g2 lds");
constexpr int LDS_BYTES = 163840;
constexpr unsigned SPIN_CAP = 1u << 16;
constexpr int AUX_SC1 = 16;
#define POLL_GAP 2
#define POLL_GAP0 2
#define CTR 0
#define XW 0
#define XA 0
#define XX 0
#define XO 0
#define MPRIO 3
#define MPRIO2 0
#define WDS 12
static_assert(WDS >= 11, "the wait counts assume the request behind the list");
#define PREP_I 8
#define AT_I 10
#define CONV_PRIO 1
#define SFLAG 5
#define AUX_OUT 17
#define ROT (8 * pg + 2 * ((mp >> 1) & 3))

struct Params {
    const u16* x; const u16* logA; const u16* Aim; const u16* Cre; const u16* Cim; const u16* logdt; const u16* Dskip;
    const u16* wg; const u16* wu; const u16* wd; u16* out; unsigned char* ws;
};

DI float bf2f(unsigned b) { return __uint_as_float(b << 16); }
DI unsigned pk_bf(float a, float b) { f32x2 v = {a, b}; return __builtin_bit_cast(unsigned, __builtin_convertvector(v, bf16x2)); }
DI unsigned f2bf(float f) { return pk_bf(f, 0.f) & 0xFFFFu; }
DI unsigned pk_f16(float a, float b) { f32x2 v = {a, b}; return __builtin_bit_cast(unsigned, __builtin_convertvector(v, f16x2)); }
DI float rbf(float f) { return bf2f(f2bf(f)); }
#define DPPF(v, ctrl) __builtin_bit_cast(float, __builtin_amdgcn_update_dpp(0, __builtin_bit_cast(int, (v)), (ctrl), 0xF, 0xF, true))
DI float rdl(float v, int l) { return __builtin_bit_cast(float, __builtin_amdgcn_readlane(__builtin_bit_cast(int, v), l)); }
DI float wave_sum(float v) {
    v += DPPF(v, 0xB1); v += DPPF(v, 0x4E); v += DPPF(v, 0x141); v += DPPF(v, 0x140);
    return (rdl(v, 0) + rdl(v, 16)) + (rdl(v, 32) + rdl(v, 48));
}
DI float wave_max(float v) {
    v = fmaxf(v, DPPF(v, 0xB1)); v = fmaxf(v, DPPF(v, 0x4E)); v = fmaxf(v, DPPF(v, 0x141)); v = fmaxf(v, DPPF(v, 0x140));
    return fmaxf(fmaxf(rdl(v, 0), rdl(v, 16)), fmaxf(rdl(v, 32), rdl(v, 48)));
}
DI rsrc_t mk_rsrc(const void* p, unsigned bytes) { return __builtin_amdgcn_make_buffer_rsrc((void*)p, 0, bytes, 0x00020000); }
DI void dma16(unsigned lds, unsigned voff, rsrc_t rs, unsigned soff) {
    asm volatile("s_mov_b32 m0, %0\n\ts_nop 0\n\tbuffer_load_dwordx4 %1, %2, %3 offen lds" :: "s"(lds), "v"(voff), "s"(rs), "s"(soff) : "memory");
}
DI void dma16_sc1(unsigned lds, unsigned voff, rsrc_t rs, unsigned soff) {
    asm volatile("s_mov_b32 m0, %0\n\ts_nop 0\n\tbuffer_load_dwordx4 %1, %2, %3 offen sc1 lds" :: "s"(lds), "v"(voff), "s"(rs), "s"(soff) : "memory");
}
DI void dma4_sc1(unsigned lds, unsigned voff, rsrc_t rs, unsigned soff) {
    asm volatile("s_mov_b32 m0, %0\n\ts_nop 0\n\tbuffer_load_dword %1, %2, %3 offen sc1 lds" :: "s"(lds), "v"(voff), "s"(rs), "s"(soff) : "memory");
}
DI u32x4 ld16_sc1(unsigned voff, rsrc_t rs) {
    u32x4 r;
    asm volatile("buffer_load_dwordx4 %0, %1, %2, 0 offen sc1" : "=v"(r) : "v"(voff), "s"(rs) : "memory");
    return r;
}
DI unsigned ld4_sc1(unsigned voff, rsrc_t rs) {
    unsigned r;
    asm volatile("buffer_load_dword %0, %1, %2, 0 offen sc1" : "=v"(r) : "v"(voff), "s"(rs) : "memory");
    return r;
}
#define WAITV(n) asm volatile("s_waitcnt vmcnt(%0)" :: "i"(n) : "memory")
#define WAITL() asm volatile("s_waitcnt lgkmcnt(0)" ::: "memory")
#define SB0() __builtin_amdgcn_sched_barrier(0)
#define BAR() do { SB0(); asm volatile("s_barrier" ::: "memory"); SB0(); } while (0)
#define BARL() do { SB0(); asm volatile("s_waitcnt lgkmcnt(0)\n\ts_barrier" ::: "memory"); SB0(); } while (0)
template <class T> DI T lds_ld(unsigned a) { return *(const LAS T*)(size_t)a; }
template <class T> DI void lds_st(unsigned a, T v) { *(LAS T*)(size_t)a = v; }
DI s16x8 lds_tr2(unsigned a0, unsigned a1) {
    s16x4 lo = __builtin_amdgcn_ds_read_tr16_b64_v4i16((LAS s16x4*)(size_t)a0);
    s16x4 hi = __builtin_amdgcn_ds_read_tr16_b64_v4i16((LAS s16x4*)(size_t)a1);
    return __builtin_shufflevector(lo, hi, 0, 1, 2, 3, 4, 5, 6, 7);
}
DI void grp_bar(unsigned cnt, unsigned target, int lane) {
    SB0();
    asm volatile("s_waitcnt lgkmcnt(0)" ::: "memory");
    if (lane == 0) { const unsigned one = 1; asm volatile("ds_add_u32 %0, %1" :: "v"(cnt), "v"(one) : "memory"); }
    #pragma unroll 1
    for (unsigned it = 0; it < (1u << 22); ++it) {
        unsigned c;
        asm volatile("ds_read_b32 %0, %1\n\ts_waitcnt lgkmcnt(0)" : "=v"(c) : "v"(cnt) : "memory");
        if (__builtin_amdgcn_readfirstlane(c) >= target) break;
    }
    SB0();
}
#define MFMA_BF(a, b, c) __builtin_amdgcn_mfma_f32_32x32x16_bf16((a), (b), (c), 0, 0, 0)
#define MFMA_H(a, b, c) __builtin_amdgcn_mfma_f32_32x32x16_f16(__builtin_bit_cast(f16x8, a), __builtin_bit_cast(f16x8, b), (c), 0, 0, 0)
#define RLX_AGENT __ATOMIC_RELAXED, __HIP_MEMORY_SCOPE_AGENT
DI unsigned ld_sc1_u32(const unsigned char* p) { return __hip_atomic_load((const GAS unsigned*)p, RLX_AGENT); }
DI void st_sc1_u32(unsigned char* p, unsigned v) { __hip_atomic_store((GAS unsigned*)p, v, RLX_AGENT); }
DI bool spin_fail(unsigned& spins, unsigned char* ws, unsigned tag) {
    __builtin_amdgcn_s_sleep(1);
    if (++spins > SPIN_CAP) { st_sc1_u32(ws + WS_TMO, tag); return true; }
    return (spins & 255u) == 0u && __builtin_amdgcn_readfirstlane(ld_sc1_u32(ws + WS_TMO)) == tag;
}
DI void wait_flags(unsigned char* ws, unsigned off, int nw, unsigned tag, int lane) {
    for (unsigned spins = 0;;) {
        unsigned v = tag;
        if (lane < nw) v = ld_sc1_u32(ws + off + 4 * lane);
        if (__all(v == tag)) return;
        if (spin_fail(spins, ws, tag)) return;
    }
}
DI void lds_arrive(unsigned cnt) {
    const unsigned one = 1;
    asm volatile("s_mov_b64 exec, 1\n\tds_add_u32 %0, %1\n\ts_mov_b64 exec, -1" :: "v"(cnt), "v"(one) : "memory");
}
DI void lds_wait_ge(unsigned cnt, unsigned target) {
    SB0();
    #pragma unroll 1
    for (unsigned it = 0; it < (1u << 22); ++it) {
        unsigned c;
        asm volatile("ds_read_b32 %0, %1\n\ts_waitcnt lgkmcnt(0)" : "=v"(c) : "v"(cnt) : "memory");
        if (__builtin_amdgcn_readfirstlane(c) >= target) break;
        __builtin_amdgcn_s_sleep(1);
    }
    SB0();
}
DI void wait_flags4(unsigned& p0, unsigned& p1, unsigned& p2, unsigned& p3, unsigned char* ws, rsrc_t rs, unsigned vo, unsigned tag) {
    unsigned n;
#define POLL_STEP(NEW, OLD) "buffer_load_dword " NEW ", %5, %6, 0 offen sc1\n\ts_waitcnt vmcnt(3)\n\tv_cmp_ne_u32 vcc, %7, " OLD "\n\ts_cbranch_vccz 2f\n\ts_sleep %9\n\t"
    asm volatile(
        "buffer_load_dword %0, %5, %6, 0 offen sc1\n\ts_sleep %8\n\t"
        "buffer_load_dword %1, %5, %6, 0 offen sc1\n\ts_sleep %8\n\t"
        "buffer_load_dword %2, %5, %6, 0 offen sc1\n\ts_sleep %8\n\t"
        "s_mov_b32 %4, 0\n"
        "1:\n\t"
        POLL_STEP("%3", "%0") POLL_STEP("%0", "%1") POLL_STEP("%1", "%2") POLL_STEP("%2", "%3")
        "s_add_u32 %4, %4, 1\n\ts_cmp_lt_u32 %4, %10\n\ts_cbranch_scc1 1b\n"
        "2:"
        : "=&v"(p0), "=&v"(p1), "=&v"(p2), "=&v"(p3), "=&s"(n) : "v"(vo), "s"(rs), "s"(tag), "i"(POLL_GAP0), "i"(POLL_GAP), "i"(SPIN_CAP / 4) : "vcc", "scc", "memory");
#undef POLL_STEP
    if (n >= SPIN_CAP / 4) st_sc1_u32(ws + WS_TMO, tag);
}
#define STAMP(i) do { } while (0)
#define STAMPC(i) do { } while (0)
#define STAMP_DUMP() do { } while (0)

#define TICK_DECL2
#define TICK2(i) do { } while (0)
#define TICK2_DUMP(n, blk) do { } while (0)
#define TICK3(i) do { } while (0)
#define TICK_DECL
#define TICK(i) do { } while (0)
#define TICK_DUMP(n, blk) do { } while (0)
#define HT_KIND 0
#define MT_KIND 0
#define TKM 0
#define TKH 8
template <int I> struct IC { static constexpr int value = I; };
template <int I, int N, class F> DI void sfor(F&& f) { if constexpr (I < N) { f(IC<I>{}); sfor<I + 1, N>(f); } }
DI bool all_eq_hp(unsigned a, unsigned b_uniform) {
    unsigned long long m;
    asm volatile("s_setprio 1\n\tv_cmp_ne_u32 %0, %1, %2\n\ts_setprio 0" : "=s"(m) : "s"(b_uniform), "v"(a));
    return m == 0;
}
typedef f32x2 C2;
DI void cmul2(C2& r0, const C2 a0, const C2 b0, C2& r1, const C2 a1, const C2 b1) {
    float x0, y0, x1, y1;
    const float a0x = a0.x, a0y = a0.y, b0x = b0.x, b0y = b0.y, a1x = a1.x, a1y = a1.y, b1x = b1.x, b1y = b1.y;
    asm volatile("v_mul_f32 %0, %4, %6\n\tv_mul_f32 %1, %4, %7\n\tv_mul_f32 %2, %8, %10\n\tv_mul_f32 %3, %8, %11\n\t"
        "v_fma_f32 %0, -%5, %7, %0\n\tv_fmac_f32 %1, %5, %6\n\tv_fma_f32 %2, -%9, %11, %2\n\tv_fmac_f32 %3, %9, %10"
        : "=&v"(x0), "=&v"(y0), "=&v"(x1), "=&v"(y1)
        : "v"(a0x), "v"(a0y), "v"(b0x), "v"(b0y), "v"(a1x), "v"(a1y), "v"(b1x), "v"(b1y));
    r0 = C2{x0, y0}; r1 = C2{x1, y1};
}
DI C2 cexp_p(float ar2, float th, float p) {
    const float m = __builtin_amdgcn_exp2f(ar2 * p), t = __builtin_amdgcn_fractf(th * p);
    const float c = __builtin_amdgcn_cosf(t), s = __builtin_amdgcn_sinf(t);
    float x, y;
    asm volatile("s_nop 0\n\tv_mul_f32 %0, %2, %3\n\tv_mul_f32 %1, %2, %4" : "=&v"(x), "=&v"(y) : "v"(m), "v"(c), "v"(s));
    return C2{x, y};
}

struct G1A { unsigned bx0, bx1, bx2, bx3, bw0, bw1, vo_cs, vo_fp, vo_pf, a_cs, a_st, a_cw; };
template <int NU, class Post>
DI void gemm1_loop(f32x16 (&acc)[3], u32x4 (&csf)[3][2], unsigned long long& gran, const G1A& ga, unsigned& tag, Post post, int v, int tb, int mp, int lane, rsrc_t rs_ws, rsrc_t rs_wd, unsigned vo_fxp, unsigned char* ws) {
    const unsigned bx[4] = {ga.bx0, ga.bx1, ga.bx2, ga.bx3}, bw[2] = {ga.bw0, ga.bw1};
    auto rdx = [&](int kg) { return lds_ld<s16x8>(bx[kg & 3] + ((kg >> 2) & 3) * XSLOT); };
    auto rdw = [&](int kg, int uu) { const int s = (kg >> 1) % 6; const unsigned a = bw[s / 3] + (s % 3) * WSLOT + (kg & 1) * 3072 + 32 * uu; return lds_tr2(a, a + 768); };
    u32x4 ones = {0x3F803F80u, 0x3F803F80u, 0x3F803F80u, 0x3F803F80u};
    asm volatile("" : "+v"(ones));
    const unsigned long long cs_exec = tb < NU ? 0xFFFFFFFFull : 0ull;
    f32x16 cs;
    float cs_h, cs_r;
    unsigned cs_p;
    const unsigned vo_cs = ga.vo_cs, vo_fp = ga.vo_fp, vo_pf = ga.vo_pf;
    s16x8 A[3][NU], B[3];
    #pragma unroll
    for (int k0 = 0; k0 < 2; ++k0) {
        #pragma unroll
        for (int uu = 0; uu < NU; ++uu) A[k0][uu] = rdw(k0, uu);
        B[k0] = rdx(k0);
    }
    const s16x8 cw0 = lds_tr2(ga.a_cw, ga.a_cw + 768), cw1 = lds_tr2(ga.a_cw + 3072, ga.a_cw + 3840);
    SB0();
    TICK_DECL;
    TICK(0);
    unsigned vtag, pfw = 0;
    #pragma unroll
    for (int S = 0; S < 16; ++S) {
        if (S == SFLAG) {
            WAITV(0);
            if (tb < NU) asm volatile("s_setprio 3\n\ts_mov_b64 exec, 1\n\tbuffer_store_dword %0, %1, %2, 0 offen sc1\n\ts_mov_b64 exec, -1\n\ts_setprio 0" :: "v"(vtag), "v"(vo_fp), "s"(rs_ws) : "memory");
        }
        if (MT_KIND == 1) TICK(1 + S);
        if (S >= 1 && S < 14) BAR();
        if (MT_KIND == 0) TICK(1 + S);
        if (S == SFLAG && v == 0)
            asm volatile("s_setprio 3\n\ts_mov_b64 exec, 1\n\tbuffer_store_dword %0, %1, %2, 0 offen sc1\n\ts_mov_b64 exec, -1\n\ts_setprio 0" :: "v"(vtag), "v"(vo_fxp), "s"(rs_ws) : "memory");
        #pragma unroll
        for (int ks = 0; ks < 2; ++ks) {
            const int kg = 2 * S + ks, c = kg % 3, nx = (kg + 2) % 3;
            #pragma unroll
            for (int uu = 0; uu < NU; ++uu) {
                acc[uu] = MFMA_BF(A[c][uu], B[c], acc[uu]);
                SB0();
                if (kg + 2 < 32) { A[nx][uu] = rdw(kg + 2, uu); if (uu == 0) B[nx] = rdx(kg + 2); }
                if (S == 15 && ks == 0) { csf[uu][0] = lds_ld<u32x4>(ga.a_cs + uu * 2048); csf[uu][1] = lds_ld<u32x4>(ga.a_cs + uu * 2048 + 1024); if (uu == 0) gran = lds_ld<unsigned long long>(ga.a_st); }
                SB0();
                if (S == 1) {
                    const int sl = ks * NU + uu, i0 = NU == 3 ? sl - 1 : 3 * sl, i1 = NU == 3 ? sl : 3 * sl + 3;
                    #pragma unroll
                    for (int i = i0; i < i1; ++i) {
                        if (i == 0) { __builtin_amdgcn_s_setprio(3); cs_p = pk_bf(cs[0], 0.f); asm volatile("" : "+v"(cs_p)); __builtin_amdgcn_s_setprio(0); }
                        if (i == 1) asm volatile("s_setprio 3\n\tv_lshlrev_b32 %0, 16, %1\n\ts_setprio 0" : "=v"(cs_h) : "v"(cs_p));
                        if (i == 2) asm volatile("s_setprio 3\n\tv_sub_f32 %0, %1, %2\n\ts_setprio 0" : "=v"(cs_r) : "v"(cs[0]), "v"(cs_h));
                        if (i == 3) asm volatile("s_setprio 3\n\tv_cvt_pk_bf16_f32 %0, %1, %2\n\ts_setprio 0" : "=v"(cs_p) : "v"(cs_h), "v"(cs_r));
                        if (i == 4) asm volatile("s_setprio 3\n\ts_mov_b64 exec, %3\n\tbuffer_store_dword %0, %1, %2, 0 offen sc1\n\ts_mov_b64 exec, -1\n\ts_setprio 0" :: "v"(cs_p), "v"(vo_cs), "s"(rs_ws), "s"(cs_exec) : "memory");
                    }
                }
                SB0();
            }
            if (S == 0 && ks == 0) { cs = MFMA_BF(ones, cw0, f32x16{}); SB0(); }
            if (S == 0 && ks == 1) { cs = MFMA_BF(ones, cw1, cs); SB0(); }
            if (S == 0 && ks == 0) {
                post(vtag);
                tag = __builtin_amdgcn_readfirstlane(vtag);
                __builtin_amdgcn_s_setprio(0);
                SB0();
            }
        }
    }
    TICK(17);
    if (v == TKM) TICK_DUMP(18, 0);
}

#define NPRO 27
#define CAP 7
#define KPRO 12
struct HSched { int first[15]; int nw[15]; };
constexpr HSched h_sched() {
    HSched r{};
    r.first[0] = NPRO;
    for (int i = 0; i < 14; ++i) {
        const int lim = i == 0 ? 34 : (i >= 10 ? 80 : 37 + 5 * (i - 1));
        r.first[i + 1] = r.first[i] + CAP < lim ? r.first[i] + CAP : lim;
    }
    for (int S = 0; S < 14; ++S) {
        const int st = S == 13 ? 15 : S + 1, xa = st >> 1;
        const int wend = st < 6 ? 10 * (st >> 1) + 7 + 3 * (st & 1) : (st == 15 ? 80 : 37 + 5 * (st - 6));
        const int xend = xa < 4 ? 10 * xa + 4 : 37 + 5 * (2 * xa + 1 - 8) + 2;
        const int need = wend > xend ? wend : xend;
        const int extra = (S > 8 && r.first[9] >= need ? 1 : 0) + (S > 10 && r.first[11] >= need ? 1 : 0) + (S > 11 && r.first[12] >= need ? 4 : 0);
        r.nw[S] = need <= r.first[S] ? r.first[S] - need + extra : -1;
    }
    return r;
}
constexpr HSched HS = h_sched();
static_assert(HS.first[12] == 80 && NPRO >= KPRO + 2 && NPRO <= 34, "schedule");
static_assert(HS.nw[0] >= 0 && HS.nw[1] >= 0 && HS.nw[2] >= 0 && HS.nw[3] >= 0 && HS.nw[4] >= 0 && HS.nw[5] >= 0 && HS.nw[6] >= 0 && HS.nw[7] >= 0 && HS.nw[8] >= 0 && HS.nw[9] >= 0 && HS.nw[10] >= 0 && HS.nw[11] >= 0 && HS.nw[12] >= 0 && HS.nw[13] >= 0, "schedule");
#define G2HD 6
constexpr int G2_HD = G2HD;
struct G2Sched { int nw[11]; };
constexpr G2Sched g2_sched() {
    G2Sched r{};
    int wend[11] = {}, hend[11] = {}, n = 0;
    for (int i = 0; i < 5; ++i) { n += 2; wend[i] = n; }
    n += 1;
    n += 2; wend[5] = n;
    for (int i = 0; i < G2_HD; ++i) { n += 2; hend[i] = n; }
    for (int i = 0; i < 11; ++i) {
        if (i + G2_HD < 11) { n += 2; hend[i + G2_HD] = n; }
        const int wn = i + 1 < 11 ? wend[i + 1] : 0;
        r.nw[i] = n - (hend[i] > wn ? hend[i] : wn);
        if (i > 0 && i + 5 < 11) { n += 2; wend[i + 5] = n; }
    }
    return r;
}
#define CODE_BYTES 38400

#define ARGS_HERE() do { SB0(); asm volatile("" :: "s"(p.x), "s"(p.logA), "s"(p.Aim), "s"(p.Cre), "s"(p.Cim), "s"(p.logdt), "s"(p.Dskip), "s"(p.wg), "s"(p.wu), "s"(p.wd), "s"(p.out), "s"(p.ws)); SB0(); } while (0)
__global__ __launch_bounds__(768, 3) void fused_kernel(Params p) {
    extern __shared__ __attribute__((aligned(16))) unsigned char lds_raw[];
    const int hwv = __builtin_amdgcn_readfirstlane(threadIdx.x >> 6);
    const int v = hwv < 4 ? hwv + 8 : hwv - 4;
    const int lane = threadIdx.x & 63, tid = 64 * v + lane;
    const int w = blockIdx.x, g = w & 7, j = w >> 3;
    const int pg = g & 1, gg = g >> 1;
    const int mp = 8 * pg + (j & 7), ng = gg + 4 * (j >> 3);
    const int U0 = 6 * ng - 2 * max(ng - 11, 0);
    const int nunits = 6 - 2 * min(max(ng - 10, 0), 1);
    const int tb8 = 128 * pg + 32 * gg + j;
    const int d0 = 64 * g + 2 * j;
    const int rt = 4 * g + (j >> 3), ct = (j + CTR) & 7;
    const int r32 = lane & 31, hh = lane >> 5;
    const int G = lane >> 4, q4 = (lane >> 2) & 3, p4 = lane & 3, gm = G & 1;
    STAMP(0); STAMPC(30);
    unsigned pfc = 0;
    if (hwv == 0) {
        unsigned long long pc;
        asm volatile("s_getpc_b64 %0" : "=s"(pc));
        pc &= ~127ull;
        const unsigned ln = j % 5 + 5 * lane, vo2 = (ln < CODE_BYTES / 128 ? ln : 0) * 128;
        asm volatile("global_load_dword %0, %1, %2" : "=v"(pfc) : "v"(vo2), "s"(pc) : "memory");
    }

    if (v >= 8) {
        const int hv = v - 8;
        const int ch = hv >> 1, nh = hv & 1, d = d0 + ch;
        __builtin_amdgcn_s_setprio(2);
        constexpr HSched hs = h_sched();
        const int a = hv & 1, hb = hv >> 1;
        unsigned vo_x[2], vo_w[3], vo_p, a_par = L_PAR + hv * 1024 + (64 * nh + lane) * 2, a_par1 = L_PAR + hv * 1024 + (64 * (nh ^ 1) + lane) * 2;
        #pragma unroll
        for (int e = 0; e < 2; ++e) vo_x[e] = (lane >> 3) * 1024 + 16 * ((lane & 7) ^ ((4 * e + (lane >> 4)) & 7));
        #pragma unroll
        for (int i = 0; i < 3; ++i) { const int c = 64 * i + lane, row = c / 12, col = c - 12 * row; vo_w[i] = row * (F_ * 2) + col * 16; }
        vo_p = d * (NS_ * 2) + (lane & 15) * 16;
        asm volatile("" : "+v"(vo_x[0]), "+v"(vo_x[1]), "+v"(vo_w[0]), "+v"(vo_w[1]), "+v"(vo_w[2]), "+v"(vo_p), "+v"(a_par), "+v"(a_par1));
        SB0();
        ARGS_HERE();
        unsigned char* ws = p.ws;
        const rsrc_t rs_ws = mk_rsrc(ws, WS_END);
        const rsrc_t rs_x = mk_rsrc(p.x, L_ * D_ * 2);
        const rsrc_t rs_w = mk_rsrc(a ? p.wu : p.wg, D_ * F_ * 2);
        const int rot = ROT;
        auto xdma = [&](int xa, int hf, int i) {
            const unsigned ko = 128 * ((xa + (rot >> 1)) & 7);
            dma16((xa & 3) * XSLOT + (8 * hf + 4 * a + 2 * hb + i) * 1024, vo_x[i], rs_x, (unsigned)((128 * mp + 64 * hf + 32 * a + 16 * hb + 8 * i) * 1024) + ko);
        };
        auto wdma = [&](int S, int i) { dma16(W_RING + (S % 6) * WSLOT + a * W_M + (3 * hb + i) * 1024, vo_w[i], rs_w, (unsigned)((32 * ((S + rot) & 15) + 16 * hb) * (F_ * 2) + 32 * U0)); };
        auto dma_i = [&](int d) {
            if (d < 34) { const int blk = d / 10, r = d % 10; if (r < 4) xdma(blk, r >> 1, r & 1); else wdma(2 * blk + (r - 4) / 3, (r - 4) % 3); }
            else if (d < 37) wdma(6, d - 34);
            else {
                const int S = 2 + (d - 37) / 5, r = (d - 37) % 5;
                if (S == 10) wdma(15, r); else if (r < 2) xdma((S + 6) >> 1, (S + 6) & 1, r); else wdma(S + 5, r - 2);
            }
        };
        {
            const unsigned char* pb = (const unsigned char*)(G < 2 ? (G == 0 ? p.logA : p.Aim) : (G == 2 ? p.Cre : p.Cim)) + vo_p;
            asm volatile("s_mov_b32 m0, %0\n\ts_nop 0\n\tglobal_load_lds_dwordx4 %1, off" :: "s"(L_PAR + hv * 1024), "v"(pb) : "memory");
        }
        unsigned wdt, wsk, pdt, dskb;
        asm volatile("s_load_dword %0, %2, %4\n\ts_load_dword %1, %3, %4" : "=&s"(wdt), "=&s"(wsk) : "s"(p.logdt), "s"(p.Dskip), "s"((d >> 1) * 4));
        unsigned pa, pi, pcr0, pci0, pcr1, pci1;
        C2 lo16[16], hi16[16], hj16[16], hk[9], m16, m1, m2, m4, m8, m256, b1024, b1280;
        unsigned PL[16], P0[16], P1[16], P2[9];
        float isc_out = 0.f, k_dt = 0.f, k_mx = 0.f, k_skip = 0.f;
        auto pack4 = [&](unsigned* P, const C2* z, int r0) {
            #pragma unroll
            for (int i = r0; i < r0 + 4; ++i) { P[i] = pk_f16(z[i].x, z[i].y); asm volatile("" : "+v"(P[i])); }
        };
        auto kslot = [&](int n) {
            SB0();
            if (n == 0) { k_dt = rbf(__expf(bf2f(pdt))); m16.x = -rbf(__expf(bf2f(pa))); asm volatile("" : "+v"(m16), "+v"(k_dt)); }
            if (n == 1) {
                m16 = C2{m16.x * k_dt * 1.4426950408889634f, bf2f(pi) * k_dt * 0.15915494309189535f};
                hi16[0] = C2{bf2f(pcr0) * k_dt, bf2f(pci0) * k_dt};
                asm volatile("" : "+v"(m16), "+v"(hi16[0]));
            }
            if (n == 2) {
                k_mx = fmaxf(fmaxf(fmaxf(fabsf(hi16[0].x), fabsf(hi16[0].y)), fmaxf(fabsf(bf2f(pcr1) * k_dt), fabsf(bf2f(pci1) * k_dt))), fabsf(bf2f(dskb)) * (1.f / 128.f));
                asm volatile("" : "+v"(k_mx));
            }
            if (n == 3) { k_mx = wave_max(k_mx); asm volatile("" : "+v"(k_mx)); }
            if (n == 4) {
                int e = (int)((__float_as_uint(k_mx) >> 23) & 0xFF);
                e = e < 10 ? 10 : (e > 240 ? 240 : e);
                const float scale2 = __uint_as_float((unsigned)(260 - e) << 23);
                isc_out = __uint_as_float((unsigned)(e - 5) << 23);
                hi16[0] = C2{hi16[0].x * scale2, -hi16[0].y * scale2};
                k_skip = (nh == 0 && lane == 0) ? bf2f(dskb) * (0.5f * scale2) : 0.f;
                asm volatile("" : "+v"(hi16[0]), "+v"(isc_out), "+v"(k_skip));
            }
            if (n == 5) m1 = cexp_p(m16.x, -m16.y, 1.f);
            if (n == 6) m256 = cexp_p(m16.x, -m16.y, 256.f);
            if (n == 7) { m16 = cexp_p(m16.x, m16.y, 16.f); lo16[0] = C2{1.f, 0.f}; lo16[1] = m16; }
            if (n >= 8 && n <= 22) {
                const int i = n - 7;
                if (i <= 14) cmul2(lo16[i + 1], lo16[i], m16, hi16[i], hi16[i - 1], m1);
                else cmul2(hj16[0], hi16[0], m256, hi16[15], hi16[14], m1);
            }
            if (n == 23) cmul2(m2, m1, m1, hj16[8], hj16[0], m256);
            if (n == 24) cmul2(m4, m2, m2, hj16[12], hj16[8], m256);
            if (n == 25) cmul2(m8, m4, m4, b1024, hj16[12], m256);
            if (n >= 26 && n <= 29) { pack4(PL, lo16, 4 * (n - 26)); pack4(P0, hi16, 4 * (n - 26)); }
            if (n >= 30 && n <= 32) cmul2(hj16[n - 29], hj16[n - 30], m2, hj16[n - 21], hj16[n - 22], m4);
            if (n >= 33 && n <= 35) cmul2(hj16[n - 29], hj16[n - 30], m2, hj16[n - 20], hj16[n - 21], m4);
            if (n == 36) { cmul2(hj16[7], hj16[6], m2, b1280, b1024, m256); hk[0] = b1024; hk[2] = b1280; }
            if (n == 37) cmul2(hk[4], b1280, m256, hk[1], b1024, m8);
            if (n == 38) cmul2(hk[6], hk[4], m256, hk[3], b1280, m8);
            if (n == 39) cmul2(hk[8], hk[6], m256, hk[5], hk[4], m8);
            if (n == 40) { C2 dm; cmul2(hk[7], hk[6], m8, dm, hk[6], m8); }
            if (n == 41 || n == 42) { pack4(P1, hj16, 8 * (n - 41)); pack4(P1, hj16, 8 * (n - 41) + 4); }
            if (n == 43) { pack4(P2, hk, 0); pack4(P2, hk, 4); P2[8] = pk_f16(hk[8].x, hk[8].y); asm volatile("" : "+v"(P2[8])); }
            SB0();
        };
        sfor<0, NPRO>([&](auto dc) {
            constexpr int dd = decltype(dc)::value;
            dma_i(dd);
            if (dd == KPRO - 1) {
                WAITV(KPRO);
                asm volatile("s_waitcnt lgkmcnt(0)" : "+s"(wdt), "+s"(wsk));
                pdt = ch ? wdt >> 16 : wdt & 0xFFFFu; dskb = ch ? wsk >> 16 : wsk & 0xFFFFu;
                pa = lds_ld<u16>(a_par); pi = lds_ld<u16>(a_par + 256); pcr0 = lds_ld<u16>(a_par + 512); pci0 = lds_ld<u16>(a_par + 768);
                pcr1 = lds_ld<u16>(a_par1 + 512); pci1 = lds_ld<u16>(a_par1 + 768);
                SB0();
            }
            if (dd > KPRO) kslot(dd - KPRO - 1);
        });
        WAITV(hs.nw[0]);
        asm volatile("" : "+v"(pfc));
        if (tid == 512) lds_st<u32x4>(L_CNT, u32x4{0u, 0u, 0u, 0u});
        BARL();
        STAMP(4);
        const unsigned tagv = lds_ld<unsigned>(L_SCL + 8);
        constexpr int KP0 = NPRO - KPRO - 1, KPI = (44 - KP0 + 10) / 11;
        unsigned fcH, fxH;
        unsigned vo_fc = WS_FLAGC + ((ng * 6 + ((3 * hv) >> 1)) * 16 + (r32 < 16 || ((3 * hv + 2) >> 1) < nunits ? r32 : r32 - 16)) * 4, vo_fx = WS_FLAGX + 4 * (64 * hv + lane);
        if (((3 * hv) >> 1) >= nunits) vo_fc = WS_FLAGC + (ng * 6 * 16 + (lane & 15)) * 4;
        unsigned vo_l16 = lane * 16, vo_l4 = lane * 4;
        asm volatile("" : "+v"(vo_fc), "+v"(vo_fx), "+v"(vo_l16), "+v"(vo_l4));
        unsigned tag = 0;
        u32x4 rc[2], rn[2], re;
        unsigned pfw = 0u;
        auto wd_l2 = [&]() {
            const rsrc_t rs_wd = mk_rsrc(p.wd, D_ * F_ * 2);
            const unsigned cs = (3u - ((unsigned)(size_t)p.wd >> 7)) & 7u, k = 11 * hv + lane;
            const unsigned o = lane < 11 && k < 43 ? (43 * j + k) * 1024 + 128 * cs : 0x7FFFFFF0u;
            asm volatile("buffer_load_dword %0, %1, %2, 0 offen" : "=v"(pfw) : "v"(o), "s"(rs_wd) : "memory");
        };
        TICK_DECL;
        TICK(0);
        sfor<0, 13>([&](auto Sc) {
            constexpr int S = decltype(Sc)::value;
            if (S > 0) {
                if (HT_KIND == 1) TICK(S);
                WAITV(hs.nw[S] + (S > WDS && WDS < 13 ? 1 : 0));
                if (HT_KIND == 2) TICK(S);
                BAR();
                if (HT_KIND == 0) TICK(S);
            }
            constexpr int n0 = KP0 + KPI * S < 44 ? KP0 + KPI * S : 44, nk = n0 + KPI <= 44 ? KPI : 44 - n0, nd = hs.first[S + 1] - hs.first[S];
            sfor<0, (nd > nk ? nd : nk)>([&](auto ic) {
                constexpr int i = decltype(ic)::value;
                if (i < nd) dma_i(hs.first[S] + i);
                if (i < nk) kslot(n0 + i);
            });
            if (S == 0) { tag = __builtin_amdgcn_readfirstlane(tagv); asm volatile("" : "+s"(tag)); }
            if (S == 8) fcH = ld4_sc1(vo_fc, rs_ws);
            if (S == 10) { fxH = ld4_sc1(vo_fx, rs_ws); STAMP(5); }
            if (S == 11) {
                WAITV(hs.first[12] - hs.first[9] + 1); asm volatile("" : "+v"(fcH));
                if (!__all(fcH == tag))
                    for (unsigned spins = 0; !spin_fail(spins, ws, tag);) { fcH = ld_sc1_u32(ws + vo_fc); if (__all(fcH == tag)) break; }
                #pragma unroll
                for (int i = 0; i < 3; ++i) {
                    const int idx = 3 * hv + i;
                    dma16_sc1(L_CSF + idx * 1024, vo_l16, rs_ws, WS_CS + (ng * 12 + (idx < 2 * nunits ? idx : 0)) * 1024);
                }
                dma4_sc1(L_STT + hv * 256, vo_l4, rs_ws, WS_STATS + (128 * mp + 32 * hv) * 8);
            }
            if (S == WDS) wd_l2();
        });
        if (HT_KIND == 1) TICK(13);
        WAITV(WDS < 13 ? 1 : 0);
        asm volatile("" : "+v"(fxH));
        if (!__all(fxH == tag))
            for (unsigned spins = 0; !spin_fail(spins, ws, tag);) { fxH = ld_sc1_u32(ws + vo_fx); if (__all(fxH == tag)) break; }
        if (HT_KIND == 2) TICK(13);
        BAR();
        if (HT_KIND == 0) TICK(13);
        if (v == TKH) TICK_DUMP(14, 1);
        {
            const unsigned xb = WS_XNT + (d0 >> 3) * 32768 + ((d0 & 7) + hh) * 16;
            #pragma unroll
            for (int i = 0; i < 2; ++i) {
                const int tbk = 64 * hv + 32 * i + r32;
                const unsigned o0 = xb + tbk * 128, o1 = tbk < 255 ? o0 + 128 : 0x7FFFFFF0u;
                asm volatile("buffer_load_dwordx4 %0, %1, %2, 0 offen sc1" : "=v"(rc[i]) : "v"(o0), "s"(rs_ws) : "memory");
                asm volatile("buffer_load_dwordx4 %0, %1, %2, 0 offen sc1" : "=v"(rn[i]) : "v"(o1), "s"(rs_ws) : "memory");
            }
            const unsigned oe = r32 == 3 ? xb : 0x7FFFFFF0u;
            asm volatile("buffer_load_dwordx4 %0, %1, %2, 0 offen sc1" : "=v"(re) : "v"(oe), "s"(rs_ws) : "memory");
        }
        #pragma unroll
        for (int e = 0; e < 2; ++e) __builtin_amdgcn_raw_buffer_store_b128(u32x4{~0u, ~0u, ~0u, ~0u}, rs_ws, WS_AT + d0 * 4096 + (2 * hv + e) * 1024 + lane * 16, 0, AUX_SC1);
        if (WDS == 13) wd_l2();
        if (w == 0 && tid == 512) st_sc1_u32(ws + WS_EPOCH, tag);
        const unsigned img = L_KG + hv * KGW;
        const int fq = lane >> 4, fr = lane & 15;
        unsigned fa = img + fr * 272 + fq * 16;
        unsigned pt0 = img + 4 * (16 * fr + 4 * fq), pt1 = img + 4 * (fq < 2 ? 256 + 8 * fr + 4 * fq : (fq == 2 ? 384 : 448) + 4 * fr);
        unsigned pt2 = img + 4 * (fq < 2 ? 512 + 64 * fq + 2 * fr : ((fq == 2 && fr == 0) ? 640 : 644)), pt3 = fq < 2 ? pt2 + 128 : img + 4 * 644;
        asm volatile("" : "+v"(fa), "+v"(pt0), "+v"(pt1), "+v"(pt2), "+v"(pt3));
        TICK_DECL2; TICK2(0);
        WAITV(WDS == 13 ? 3 : 2);
        #pragma unroll
        for (int i = 0; i < 2; ++i) asm volatile("" : "+v"(rc[i]), "+v"(rn[i]));
        asm volatile("" : "+v"(re));
        u32x4 us0[2], us1[2], us2[2], us3[2];
        #pragma unroll
        for (int i = 0; i < 2; ++i) {
            const u32x4 c = rc[i], n = rn[i];
            us0[i] = u32x4{__builtin_amdgcn_alignbyte(c[1], c[0], 2), __builtin_amdgcn_alignbyte(c[2], c[1], 2), __builtin_amdgcn_alignbyte(c[3], c[2], 2), __builtin_amdgcn_alignbyte(n[0], c[3], 2)};
            us1[i] = u32x4{c[1], c[2], c[3], n[0]};
            us2[i] = u32x4{us0[i][1], us0[i][2], us0[i][3], __builtin_amdgcn_alignbyte(n[1], n[0], 2)};
            us3[i] = u32x4{c[2], c[3], n[0], n[1]};
            asm volatile("" : "+v"(us0[i]), "+v"(us1[i]), "+v"(us2[i]), "+v"(us3[i]));
        }
        u32x4 ue0 = {0u, 0u, 0u, re[0] << 16}, ue1 = {0u, 0u, 0u, re[0]}, ue2 = {0u, 0u, re[0] << 16, __builtin_amdgcn_alignbyte(re[1], re[0], 2)}, ue3 = {0u, 0u, re[0], re[1]};
        asm volatile("" : "+v"(ue0), "+v"(ue1), "+v"(ue2), "+v"(ue3));
        unsigned ub = L_U + hh * 4 * UCP + 16 * (4 + 64 * hv + r32), ub3 = (hh ? L_ULAST : L_U + 3 * UCP) + 16 * (4 + 64 * hv + r32);
        const int aq = 255 - (64 * hv + lane);
        unsigned kra = L_KR + (lane & 3) * KRS + 16 * (lane >> 2) + 256 * hv;
        unsigned kia = L_KG + 4 * (hv < 2 ? 384 + aq : (hv == 2 ? 256 + 2 * aq : (aq >= 32 ? 128 + 4 * aq : 8 * aq)));
        asm volatile("" : "+v"(ub), "+v"(ub3), "+v"(kra), "+v"(kia));
        auto ucopy = [&]() {
            #pragma unroll
            for (int i = 0; i < 2; ++i) {
                lds_st<u32x4>(ub + 512 * i, us0[i]);
                lds_st<u32x4>(ub + 512 * i + UCP, us1[i]);
                lds_st<u32x4>(ub + 512 * i + 2 * UCP, us2[i]);
                lds_st<u32x4>(ub3 + 512 * i, us3[i]);
            }
            if (hv == 0 && r32 < 4) {
                lds_st<u32x4>(ub - 64, ue0); lds_st<u32x4>(ub - 64 + UCP, ue1); lds_st<u32x4>(ub - 64 + 2 * UCP, ue2); lds_st<u32x4>(ub3 - 64, ue3);
            }
        };
        TICK2(1);
        lds_wait_ge(C_M, 8);
        TICK2(2);
        {
#define PUT8(P, r0) asm volatile("s_mov_b32 m0, %8\n\ts_nop 0\n\t" \
                "ds_write_addtid_b32 %0 offset:%9\n\tds_write_addtid_b32 %1 offset:%9+272\n\tds_write_addtid_b32 %2 offset:%9+544\n\tds_write_addtid_b32 %3 offset:%9+816\n\t" \
                "ds_write_addtid_b32 %4 offset:%9+1088\n\tds_write_addtid_b32 %5 offset:%9+1360\n\tds_write_addtid_b32 %6 offset:%9+1632\n\tds_write_addtid_b32 %7 offset:%9+1904" \
                :: "v"(P[r0]), "v"(P[r0 + 1]), "v"(P[r0 + 2]), "v"(P[r0 + 3]), "v"(P[r0 + 4]), "v"(P[r0 + 5]), "v"(P[r0 + 6]), "v"(P[r0 + 7]), "s"(img - 61440), "i"(61440 + (r0) * 272) : "memory")
            auto frag = [&](int ks) { return __builtin_bit_cast(f16x8, lds_ld<s16x8>(fa + 64 * ks)); };
            f16x8 lo[4], f0[4], f1[4], f2[4];
            PUT8(PL, 0); PUT8(PL, 8);
            TICK3(15);
            #pragma unroll
            for (int ks = 0; ks < 4; ++ks) lo[ks] = frag(ks);
            SB0();
            PUT8(P0, 0); PUT8(P0, 8);
            #pragma unroll
            for (int ks = 0; ks < 4; ++ks) f0[ks] = frag(ks);
            SB0();
            PUT8(P1, 0); PUT8(P1, 8);
            #pragma unroll
            for (int ks = 0; ks < 4; ++ks) f1[ks] = frag(ks);
            SB0();
            PUT8(P2, 0);
            asm volatile("s_mov_b32 m0, %1\n\ts_nop 0\n\tds_write_addtid_b32 %0 offset:%2" :: "v"(P2[8]), "s"(img - 61440), "i"(61440 + 8 * 272) : "memory");
            #pragma unroll
            for (int ks = 0; ks < 4; ++ks) f2[ks] = frag(ks);
            TICK3(16);
            SB0();
            TICK3(17);
            SB0();
            f32x4 k0 = {0.f, 0.f, 0.f, 0.f}, k1 = k0, k2 = k0;
            #pragma unroll
            for (int ks = 0; ks < 4; ++ks) k0 = __builtin_amdgcn_mfma_f32_16x16x32_f16(f0[ks], lo[ks], k0, 0, 0, 0);
            #pragma unroll
            for (int ks = 0; ks < 4; ++ks) k1 = __builtin_amdgcn_mfma_f32_16x16x32_f16(f1[ks], lo[ks], k1, 0, 0, 0);
            #pragma unroll
            for (int ks = 0; ks < 4; ++ks) k2 = __builtin_amdgcn_mfma_f32_16x16x32_f16(f2[ks], lo[ks], k2, 0, 0, 0);
            k0[0] += k_skip;
            lds_st<f32x4>(pt0, k0); lds_st<f32x4>(pt1, k1);
            lds_st<f32x2>(pt2, f32x2{k2[0], k2[1]}); lds_st<f32x2>(pt3, f32x2{k2[2], k2[3]});
        }
        STAMP(6);
        if (nh == 0 && lane == 0) lds_st<float>(L_SCL + 4 * ch, isc_out);
        TICK2(3);
        WAITV(0);
        asm volatile("" : "+v"(pfw));
        grp_bar(C_H, 4, lane);
        if (hv == 0 && lane < 8) st_sc1_u32(ws + WS_FLAGA + ((d0 >> 6) * 8 + lane) * 128 + 4 * ((d0 >> 1) & 31), tag);
        TICK2(4); TICK2(5);
        STAMP(7);
        {
            auto r1 = [&](int c2, int o, int t) { return lds_ld<float>(kia + c2 * 2 * KGW + t * KGW + 4 * o); };
            auto r4 = [&](int c2, int o, int t) { return lds_ld<f32x4>(kia + c2 * 2 * KGW + t * KGW + 4 * o); };
#define UCOPY() do { SB0(); ucopy(); SB0(); } while (0)
            u32x4 o[2];
            if (hv < 2) {
                float t[2][2][2];
                #pragma unroll
                for (int i = 0; i < 8; ++i) t[i >> 2][(i >> 1) & 1][i & 1] = r1(i >> 2, (i >> 1) & 1, i & 1);
                UCOPY();
                #pragma unroll
                for (int c2 = 0; c2 < 2; ++c2) {
                    const unsigned ab = pk_f16(t[c2][0][0] + t[c2][0][1], t[c2][1][0] + t[c2][1][1]);
                    const unsigned AA = __builtin_amdgcn_perm(ab, ab, 0x01000100u), BB = __builtin_amdgcn_perm(ab, ab, 0x03020302u), BA = __builtin_amdgcn_perm(ab, ab, 0x01000302u);
                    o[c2] = u32x4{BB, (aq & 1) ? BB : BA, AA, AA};
                }
            } else if (hv == 2) {
                float t[2][3][2];
                #pragma unroll
                for (int i = 0; i < 12; ++i) t[i / 6][(i % 6) >> 1][i & 1] = r1(i / 6, (i % 6) >> 1, i & 1);
                UCOPY();
                #pragma unroll
                for (int c2 = 0; c2 < 2; ++c2) {
                    const float A = t[c2][0][0] + t[c2][0][1], B = t[c2][1][0] + t[c2][1][1], C = t[c2][2][0] + t[c2][2][1];
                    o[c2] = u32x4{pk_f16(C, C), pk_f16(B, B), pk_f16(B, A), pk_f16(A, A)};
                }
            } else {
                f32x4 t[2][2][2];
                #pragma unroll
                for (int i = 0; i < 8; ++i) t[i >> 2][(i >> 1) & 1][i & 1] = r4(i >> 2, 4 * ((i >> 1) & 1), i & 1);
                UCOPY();
                const bool far = aq >= 32;
                #pragma unroll
                for (int c2 = 0; c2 < 2; ++c2) {
                    const f32x4 t0 = t[c2][0][0] + t[c2][0][1], t1 = t[c2][1][0] + t[c2][1][1];
                    o[c2] = u32x4{pk_f16(far ? t1[0] : t1[3], far ? t0[3] : t1[2]), pk_f16(far ? t0[2] : t1[1], far ? t0[2] : t1[0]),
                                  pk_f16(far ? t0[2] : t0[3], far ? t0[1] : t0[2]), pk_f16(far ? t0[0] : t0[1], t0[0])};
                }
            }
#undef UCOPY
            lds_st<u32x4>(kra, o[0]); lds_st<u32x4>(kra + KRB, o[1]);
            if (hv < 2 && (hv == 0 || lane < 60)) { lds_st<u32x4>(kra + 1024, u32x4{0u, 0u, 0u, 0u}); lds_st<u32x4>(kra + KRB + 1024, u32x4{0u, 0u, 0u, 0u}); }
        }
        STAMP(8);
        TICK2(6);
        grp_bar(C_H, 8, lane);
        TICK2(7);
        const int half = nh;
        const float isc = lds_ld<float>(L_SCL + 4 * ch);
        __builtin_amdgcn_s_setprio(CONV_PRIO);
        lds_wait_ge(C_GELU, 8);
        STAMP(9); STAMPC(25);
        TICK2(8);
        {
            f32x16 y0 = {}, y1 = {};
            unsigned a_u = ((ch && q4 == 3) ? L_ULAST : L_U + ch * 4 * UCP + q4 * UCP) + 2 * (8 * hh + 16 * gm + 4 * p4) + 1024 * half;
            unsigned a_k = L_KR + ch * KRB + hh * KRS + 16 * (63 - r32 + 16 * half) - 512;
            asm volatile("" : "+v"(a_u), "+v"(a_k));
            auto rdk = [&](int i, int far) { return lds_ld<s16x8>(a_k + (far ? 0 : 512) + (i & 1) * 2 * KRS + 16 * (i >> 1)); };
            auto rdu = [&](int i, int far) { return lds_tr2(a_u + 32 * i + (far ? 2048 : 0), a_u + 32 * i + (far ? 2056 : 8)); };
            s16x8 Ua[3], Ub[3], K0[3], K1[3];
            #pragma unroll
            for (int i = 0; i < 2; ++i) { Ua[i] = rdu(i, 0); K0[i] = rdk(i, 0); K1[i] = rdk(i, 1); Ub[i] = rdu(i, 1); }
            #pragma unroll
            for (int i = 0; i < 32; ++i) {
                const int c = i % 3, nx = (i + 2) % 3;
                y0 = MFMA_H(Ua[c], K0[c], y0);
                SB0();
                if (i + 2 < 32) { Ua[nx] = rdu(i + 2, 0); }
                SB0();
                y1 = MFMA_H(Ua[c], K1[c], y1);
                SB0();
                if (i + 2 < 32) { K0[nx] = rdk(i + 2, 0); K1[nx] = rdk(i + 2, 1); }
                SB0();
                y1 = MFMA_H(Ub[c], K0[c], y1);
                SB0();
                if (i + 2 < 32) { Ub[nx] = rdu(i + 2, 1); }
                SB0();
            }
            STAMP(10); STAMPC(26);
            TICK2(9);
            const unsigned rb = L_CRED + ch * CRED_W + r32 * 144 + hh * 16;
            #pragma unroll
            for (int gq = 0; gq < 4; ++gq) {
                if (half) lds_st<f32x4>(rb + gq * 32, f32x4{y0[4 * gq], y0[4 * gq + 1], y0[4 * gq + 2], y0[4 * gq + 3]});
                else lds_st<f32x4>(rb + 32 * 144 + gq * 32, f32x4{y1[4 * gq], y1[4 * gq + 1], y1[4 * gq + 2], y1[4 * gq + 3]});
            }
            TICK2(10);
            grp_bar(C_H, 12, lane);
            TICK2(11);
            const unsigned ob = rb + (half ? 32 * 144 : 0);
            const unsigned sg = L_KR + hv * 2560;
            #pragma unroll
            for (int gq = 0; gq < 4; ++gq) {
                const f32x4 o = lds_ld<f32x4>(ob + gq * 32);
                float y[4];
                #pragma unroll
                for (int i = 0; i < 4; ++i) y[i] = ((half ? y1[4 * gq + i] : y0[4 * gq + i]) + o[i]) * isc;
                lds_st<u32x2>(sg + r32 * 80 + gq * 16 + hh * 8, u32x2{pk_bf(y[0], y[1]), pk_bf(y[2], y[3])});
            }
            TICK2(12);
            #pragma unroll
            for (int i = 0; i < 2; ++i) {
                const u32x4 o = lds_ld<u32x4>(sg + (16 * i + (lane >> 2)) * 80 + (lane & 3) * 16);
                __builtin_amdgcn_raw_buffer_store_b128(o, rs_ws, WS_AT + (d0 + ch) * 4096 + half * 2048 + i * 1024 + lane * 16, 0, AUX_SC1);
            }
            TICK2(13);
        }
        STAMP(11);
        WAITL();
        lds_arrive(C_H);
        TICK2(14);
        if (v == 8) TICK2_DUMP(18, 1);
        STAMP(16); STAMPC(31);
        return;
    }

    const int tb = v & 3, ug = v >> 2;
    const bool full = ug == 0 || nunits == 6;
    const unsigned vo_xr = lane * 16;
    ARGS_HERE();
    unsigned char* ws = p.ws;
    const rsrc_t rs_ws = mk_rsrc(ws, WS_END);
    const rsrc_t rs_wd = mk_rsrc(p.wd, D_ * F_ * 2);
    u32x4 xrow;
    unsigned ep = 0;
    {
        const unsigned char* xb = (const unsigned char*)p.x + (size_t)(8 * tb8 + v) * 1024;
        asm volatile("global_load_dwordx4 %0, %1, %2" : "=v"(xrow) : "v"(vo_xr), "s"(xb) : "memory");
        if (v == 0) { const unsigned z = WS_EPOCH; asm volatile("buffer_load_dword %0, %1, %2, 0 offen sc1" : "=v"(ep) : "v"(z), "s"(rs_ws) : "memory"); }
        if (v == 2) { const unsigned o = lane < 43 ? (unsigned)((w * 43 + lane) * 128) : 0x7FFFFFF0u; asm volatile("buffer_load_dword %0, %1, %2, 0 offen" : "=v"(ep) : "v"(o), "s"(rs_wd) : "memory"); }
    }
    G1A ga;
    const unsigned stat_off = WS_STATS + (128 * mp + 32 * tb + r32) * 8;
    {
        auto bxf = [&](int ks) { return (unsigned)((32 * tb + r32) * 128 + 16 * (((2 * ks) | hh) ^ ((r32 >> 1) & 7))); };
        auto bwf = [&](int b) { return (unsigned)(W_RING + b * 3 * WSLOT + gm * W_M + (8 * hh + q4) * 192 + 96 * ug + 16 * (p4 >> 1) + 8 * (p4 & 1)); };
        ga.bx0 = bxf(0); ga.bx1 = bxf(1); ga.bx2 = bxf(2); ga.bx3 = bxf(3); ga.bw0 = bwf(0); ga.bw1 = bwf(1);
        ga.vo_cs = WS_CS + ((ng * 6 + 3 * ug + tb) * 2 + ((mp >> 2) & 1)) * 1024 + ((mp >> 3) * 32 + r32) * 16 + (mp & 3) * 4;
        ga.vo_fp = WS_FLAGC + ((ng * 6 + 3 * ug + tb) * 16 + mp) * 4;
        ga.vo_pf = lane < 43 ? (unsigned)(((8 * j + v) * 43 + lane) * 128) : 0x7FFFFFF0u;
        ga.a_cs = L_CSF + 3 * ug * 2048 + lane * 16; ga.a_st = L_STT + (32 * tb + r32) * 8;
        ga.a_cw = ga.bw0 + (mp & 1) * WSLOT + 32 * (tb < (full ? 3 : 1) ? tb : 0);
        asm volatile("" : "+v"(ga.bx0), "+v"(ga.bx1), "+v"(ga.bx2), "+v"(ga.bx3), "+v"(ga.bw0), "+v"(ga.bw1), "+v"(ga.vo_cs));
        asm volatile("" : "+v"(ga.vo_fp), "+v"(ga.vo_pf), "+v"(ga.a_cs), "+v"(ga.a_st), "+v"(ga.a_cw));
    }
    unsigned tr_a = L_LN + q4 * 1024 + (64 * v + 16 * (lane >> 4) + 4 * p4) * 2, tr_o = WS_XNT + ((64 * v + lane) >> 3) * 32768 + tb8 * 128 + (lane & 7) * 16;
    unsigned vo_sts = WS_STATS + (8 * tb8 + v) * 8, vo_fxp = WS_FLAGX + 4 * tb8, a_tag = L_SCL + 8;
    asm volatile("" : "+v"(tr_a), "+v"(tr_o), "+v"(vo_sts), "+v"(vo_fxp), "+v"(a_tag));
    SB0();
    unsigned statw;
    {
        if (v == 0 || v == 2) WAITV(1); else WAITV(0);
        asm volatile("" : "+v"(xrow));
        f32x2 xv[4];
        #pragma unroll
        for (int i = 0; i < 4; ++i) xv[i] = f32x2{bf2f(xrow[i] & 0xFFFFu), __uint_as_float(xrow[i] & 0xFFFF0000u)};
        const f32x2 s2 = (xv[0] + xv[1]) + (xv[2] + xv[3]);
        const float mean = wave_sum(s2.x + s2.y) * (1.f / 512.f);
        f32x2 q2 = {0.f, 0.f};
        #pragma unroll
        for (int i = 0; i < 4; ++i) { const f32x2 dd = xv[i] - mean; q2 = dd * dd + q2; }
        const float var = wave_sum(q2.x + q2.y) * (1.f / 512.f);
        const float mb = rbf(mean), sb = rbf(__builtin_amdgcn_sqrtf(rbf(rbf(var) + 1e-5f)));
        const float ri = __builtin_amdgcn_rcpf(sb);
        u32x4 o;
        #pragma unroll
        for (int i = 0; i < 4; ++i) {
            const f32x2 dd = xv[i] - mb;
            const unsigned a = pk_bf(dd.x, dd.y);
            const f32x2 af = {bf2f(a & 0xFFFFu), __uint_as_float(a & 0xFFFF0000u)};
            f32x2 qq = af * ri;
            qq = (af - qq * sb) * ri + qq;
            const unsigned b = pk_bf(qq.x, qq.y);
            o[i] = pk_f16(bf2f(b & 0xFFFFu), __uint_as_float(b & 0xFFFF0000u));
        }
        lds_st<u32x4>(L_LN + v * 1024 + lane * 16, o);
        statw = f2bf(mb) | (f2bf(sb) << 16);
        asm volatile("" : "+v"(statw));
        SB0();
        if (v == 0) {
            asm volatile("s_waitcnt vmcnt(0)" : "+v"(ep) :: "memory");
            if (lane == 0) lds_st<unsigned>(L_SCL + 8, ep + 1u);
        }
        STAMP(1);
    }
    BARL();
    STAMP(4); STAMPC(20);

    f32x16 acc[3] = {};
    u32x4 csf[3][2];
    unsigned long long gran = 0;
    unsigned tag;
    {
        const unsigned tagv = lds_ld<unsigned>(a_tag);
        const s16x8 trow = lds_tr2(tr_a, tr_a + 4096);
        auto post = [&](unsigned& vtag) {
            __builtin_amdgcn_s_setprio(3);
            vtag = tagv;
            const u32x2 gr = {statw, tagv};
            asm volatile("s_mov_b64 exec, 1\n\tbuffer_store_dwordx2 %0, %1, %2, 0 offen sc1\n\ts_mov_b64 exec, -1" :: "v"(gr), "v"(vo_sts), "s"(rs_ws) : "memory");
            __builtin_amdgcn_raw_buffer_store_b128(__builtin_bit_cast(u32x4, trow), rs_ws, tr_o, 0, AUX_SC1);
            SB0();
        };
        if (full) gemm1_loop<3>(acc, csf, gran, ga, tag, post, v, tb, mp, lane, rs_ws, rs_wd, vo_fxp, ws);
        else gemm1_loop<1>(acc, csf, gran, ga, tag, post, v, tb, mp, lane, rs_ws, rs_wd, vo_fxp, ws);
    }
    asm volatile("" : "+v"(ep));
    STAMP(5); STAMPC(21);
    TICK_DECL2; TICK2(0);
    lds_arrive(C_M);
    for (unsigned spins = 0; !__all((unsigned)(gran >> 32) == tag);) {
        if (spin_fail(spins, ws, tag)) break;
        gran = __hip_atomic_load((const GAS unsigned long long*)(ws + stat_off), RLX_AGENT);
    }
    TICK2(1);
    const float t_mean = bf2f((unsigned)gran & 0xFFFFu), t_s = __uint_as_float((unsigned)gran & 0xFFFF0000u), t_ri = __builtin_amdgcn_rcpf(t_s);
    const float t_s2 = t_s * t_s, gel_a = -0.10294324f * t_ri * t_ri * t_ri, gel_b = -2.3022082f * t_ri;
    {
        const unsigned nm = pk_bf(-t_mean, -t_mean);
        const s16x8 Bm = __builtin_bit_cast(s16x8, u32x4{nm, nm, nm, nm});
        #pragma unroll
        for (int uu = 0; uu < 3; ++uu)
            if (uu == 0 || full) {
                acc[uu] = MFMA_BF(__builtin_bit_cast(s16x8, csf[uu][0]), Bm, acc[uu]);
                acc[uu] = MFMA_BF(__builtin_bit_cast(s16x8, csf[uu][1]), Bm, acc[uu]);
            }
        TICK2(2);
        TICK2(3);
        #pragma unroll
        for (int uu = 0; uu < 3; ++uu) {
            u32x4 o;
            #pragma unroll
            for (int i = 0; i < 4; ++i) {
                if (uu == 0 || full) {
                    const f32x2 ga = f32x2{acc[uu][2 * i], acc[uu][2 * i + 1]}, ua = f32x2{acc[uu][8 + 2 * i], acc[uu][9 + 2 * i]};
                    const f32x2 zz = ga * (ga * ga * gel_a + gel_b);
                    const f32x2 den = f32x2{__builtin_amdgcn_exp2f(zz.x), __builtin_amdgcn_exp2f(zz.y)} * t_s2 + t_s2;
                    const f32x2 hv = ga * ua * f32x2{__builtin_amdgcn_rcpf(den.x), __builtin_amdgcn_rcpf(den.y)};
                    o[i] = pk_bf(hv.x, hv.y);
                }
            }
            if (uu == 0 || full) __builtin_amdgcn_raw_buffer_store_b128(o, rs_ws, WS_H + ((4 * mp + tb) * 86 + U0 + 3 * ug + uu) * 1024 + lane * 16, 0, AUX_SC1);
            TICK2(4 + uu);
        }
    }
    lds_arrive(C_GELU);
    STAMP(6); STAMPC(22);

    {
        const int ck = lane & 7, tk = 8 * v + 4 * ((lane >> 3) & 1) + (lane >> 4), nbl = (lane >> 2) & 1;
        __builtin_amdgcn_s_setprio(MPRIO);
        WAITV(0);
        TICK2(7);
        grp_bar(C_M, 16, lane);
        TICK2(8);
        if (tid < 8) st_sc1_u32(ws + WS_FLAGH + (mp * 8 + tid) * 128 + 4 * ng, tag);
        STAMP(7);
        const unsigned vo_d = (lane >> 3) * 1024 + 16 * ((lane & 7) ^ (((lane >> 4) & 1) << 2)), vl16 = lane * 16;
        const unsigned ring = G2R + v * G2W;
        auto unit = [&](int i) { return i < 10 || v < 6 ? 8 * i + v : 85; };
        auto wdma2 = [&](int i) {
            #pragma unroll
            for (int e = 0; e < 2; ++e) dma16(ring + (i % 5) * 2048 + e * 1024, vo_d, rs_wd, (unsigned)((16 * unit(i) + 8 * e) * 1024 + 128 * (ct ^ XW)));
        };
        const unsigned sw = ((q4 >> 1) & 1) << 2;
        unsigned bd[2];
        #pragma unroll
        for (int nb = 0; nb < 2; ++nb) { bd[nb] = ring + (4 * hh + q4) * 128 + 16 * ((4 * nb + 2 * gm + (p4 >> 1)) ^ sw) + 8 * (p4 & 1); asm volatile("" : "+v"(bd[nb])); }
        auto rdd = [&](int i, int nb) { const unsigned a = bd[nb] + (i % 5) * 2048; return lds_tr2(a, a + 1024); };
        lds_wait_ge(C_H, 8);
        #pragma unroll
        for (int i = 0; i < 5; ++i) wdma2(i);
        u32x4 xv;
        {
            const unsigned char* xb = (const unsigned char*)p.x + (size_t)(64 * rt) * 1024 + 128 * (ct ^ XX);
            asm volatile("global_load_dwordx4 %0, %1, %2" : "=v"(xv) : "v"(tk * 1024 + ck * 16), "s"(xb) : "memory");
        }
        unsigned pl0 = 0, pl1 = 0, pl2 = 0, pl3 = 0;
        if (v == 0) {
            wait_flags4(pl0, pl1, pl2, pl3, ws, rs_ws, lane >= 16 && lane < 48 ? WS_FLAGA + ((ct ^ XA) * 8 + g) * 128 + 4 * (lane - 16) : WS_FLAGH + ((rt >> 1) * 8 + ct) * 128 + 4 * (lane & 15), tag); lds_arrive(C_HF);
        }
        WAITV(9);
        s16x8 W0 = rdd(0, 0), W1 = rdd(0, 1);
        asm volatile("s_waitcnt lgkmcnt(0)" : "+v"(W0), "+v"(W1) :: "memory");
        wdma2(5);
        if (v != 0) lds_wait_ge(C_HF, 1);
        STAMP(12); STAMPC(23);
        TICK2(9);
        const unsigned hbase = WS_H + 2 * rt * 86 * 1024;
        u32x4 hf[11][2];
        auto ldh = [&](int i) {
            #pragma unroll
            for (int th = 0; th < 2; ++th) asm volatile("buffer_load_dwordx4 %0, %1, %2, %3 offen sc1" : "=v"(hf[i][th]) : "v"(vl16), "s"(rs_ws), "s"(hbase + (th * 86 + unit(i)) * 1024) : "memory");
        };
        constexpr G2Sched g2 = g2_sched();
        #pragma unroll
        for (int i = 0; i < G2_HD; ++i) ldh(i);
        __builtin_amdgcn_s_setprio(MPRIO2);
        f32x16 d00 = {}, d01 = {}, d10 = {}, d11 = {};
        u32x4 at;
        unsigned vo_att = WS_AT + (64 * (ct ^ XA) + (tid >> 3)) * 4096 + (64 * rt + 8 * (tid & 7)) * 2;
        asm volatile("" : "+v"(vo_att));
        unsigned ra[8], rb[8], a_att0, a_att1, a_atw, vo_out, avh[8];
        f32x2 xf[4];
        auto prep = [&](int s) {
            if (s == 0) {
                const unsigned ro = nbl * 1040 + (lane >> 4) * 256 + ((lane >> 3) & 1) * 128 + 32 * (lane & 3);
                #pragma unroll
                for (int q = 0; q < 8; ++q) {
                    ra[q] = ro + (v < 5 ? part_a(q) + 2048 * v : part_b(q) + 2048 * (v - 5)); rb[q] = ra[q] ^ 16;
                    asm volatile("" : "+v"(ra[q]), "+v"(rb[q]));
                }
            } else {
                asm volatile("" : "+v"(xv));
                unsigned xs[4] = {xv[0], xv[1], xv[2], xv[3]};
                asm volatile("" : "+v"(xs[0]), "+v"(xs[1]), "+v"(xs[2]), "+v"(xs[3]));
                #pragma unroll
                for (int i = 0; i < 4; ++i) {
                    const unsigned xw = nbl ? xs[i ^ 2] : xs[i];
                    xf[i] = f32x2{bf2f(xw & 0xFFFFu), __uint_as_float(xw & 0xFFFF0000u)}; asm volatile("" : "+v"(xf[i]));
                }
                a_att0 = L_ATT + (8 * ck + 4 * nbl) * 132 + nbl * 16 + tk * 2; a_att1 = L_ATT + (8 * ck + 4 - 4 * nbl) * 132 + nbl * 16 + tk * 2;
                a_atw = L_ATT + (tid >> 3) * 132 + (tid >> 8) * 16 + (tid & 7) * 16;
                vo_out = (64 * rt + tk) * 1024 + (64 * (ct ^ XO) + 8 * ck) * 2;
                asm volatile("" : "+v"(a_att0), "+v"(a_att1), "+v"(a_atw), "+v"(vo_out));
                #pragma unroll
                for (int q = 0; q < 8; ++q) { avh[q] = 0; asm volatile("" : "+v"(avh[q])); }
            }
        };
        sfor<0, 11>([&](auto ic) {
            constexpr int i = decltype(ic)::value;
            SB0();
            if (i + G2_HD < 11) ldh(i + G2_HD);
            WAITV(g2.nw[i] + (i > AT_I ? 1 : 0));
            asm volatile("" : "+v"(hf[i][0]), "+v"(hf[i][1]));
            if (i == 0) asm volatile("" :: "v"(pl0), "v"(pl1), "v"(pl2), "v"(pl3));
            if (i == 0) TICK2(10);
            const s16x8 H0 = __builtin_bit_cast(s16x8, hf[i][0]), H1 = __builtin_bit_cast(s16x8, hf[i][1]);
            s16x8 N0 = W0, N1 = W1;
            if (i < 10 || v < 6) {
                d00 = MFMA_BF(H0, W0, d00);
                SB0();
                if (i + 1 < 11) N0 = rdd(i + 1, 0);
                SB0();
                d01 = MFMA_BF(H0, W1, d01);
                SB0();
                if (i + 1 < 11) N1 = rdd(i + 1, 1);
                SB0();
                if (i > 0 && i + 5 < 11) wdma2(i + 5);
                SB0();
                d10 = MFMA_BF(H1, W0, d10);
                d11 = MFMA_BF(H1, W1, d11);
            }
            W0 = N0; W1 = N1;
            SB0();
            if (i == AT_I) at = ld16_sc1(vo_att, rs_ws);
            SB0();
            if (i == PREP_I || i == PREP_I + 1) { prep(i - PREP_I); SB0(); }
        });
        STAMP(14); STAMPC(24);
        TICK2(14);
#define PUTG(DA, DB, g, base, off) asm volatile("s_mov_b32 m0, %8\n\ts_nop 0\n\t" \
            "ds_write_addtid_b32 %0 offset:%9\n\tds_write_addtid_b32 %1 offset:%9+256\n\tds_write_addtid_b32 %2 offset:%9+512\n\tds_write_addtid_b32 %3 offset:%9+768\n\t" \
            "ds_write_addtid_b32 %4 offset:%9+1024\n\tds_write_addtid_b32 %5 offset:%9+1280\n\tds_write_addtid_b32 %6 offset:%9+1536\n\tds_write_addtid_b32 %7 offset:%9+1792" \
            :: "v"(DA[4 * (g)]), "v"(DA[4 * (g) + 1]), "v"(DA[4 * (g) + 2]), "v"(DA[4 * (g) + 3]), "v"(DB[4 * (g)]), "v"(DB[4 * (g) + 1]), "v"(DB[4 * (g) + 2]), "v"(DB[4 * (g) + 3]), \
               "s"(base), "i"(off) : "memory")
        lds_wait_ge(C_H, 16);
        asm volatile("s_nop 15\n\ts_nop 15" : "+v"(d00), "+v"(d01), "+v"(d10), "+v"(d11));
        SB0();
        if (v < 4) {
            PUTG(d00, d01, 0, ring, 0); PUTG(d00, d01, 1, ring, 2048); PUTG(d00, d01, 2, ring, 4096); PUTG(d00, d01, 3, ring, 6144); PUTG(d10, d11, 0, ring, 8192);
        } else {
            const unsigned rb = ring - PAK;
            PUTG(d00, d01, 0, rb, PAK); PUTG(d00, d01, 1, rb, PAK + 2048); PUTG(d00, d01, 2, rb, PAK + 4096); PUTG(d00, d01, 3, rb, PAK + 6144); PUTG(d10, d11, 0, rb, PAK + 8192);
        }
        if (v < 5) {
            const unsigned pb = v * 6144;
            PUTG(d10, d11, 1, pb, 0); PUTG(d10, d11, 2, pb, 2048); PUTG(d10, d11, 3, pb, 4096);
        } else {
            const unsigned pb = G2R + 8 * G2W + (v - 5) * 6144 - PBK;
            PUTG(d10, d11, 1, pb, PBK); PUTG(d10, d11, 2, pb, PBK + 2048); PUTG(d10, d11, 3, pb, PBK + 4096);
        }
#undef PUTG
        TICK2(15);
        WAITV(0);
        asm volatile("" : "+v"(at));
        TICK2(11);
        {
            auto empty = [](const u32x4 a) {
                unsigned m0, m1;
                asm("v_pk_max_u16 %0, %1, %2" : "=v"(m0) : "v"(a[0]), "v"(a[1]));
                asm("v_pk_max_u16 %0, %1, %2" : "=v"(m1) : "v"(a[2]), "v"(a[3]));
                asm("v_pk_max_u16 %0, %1, %2" : "=v"(m0) : "v"(m0), "v"(m1));
                return (m0 & 0xFFFFu) == 0xFFFFu || m0 >= 0xFFFF0000u;
            };
            if (__any(empty(at)))
                for (unsigned spins = 0; !spin_fail(spins, ws, tag);) {
                    at = __builtin_amdgcn_raw_buffer_load_b128(rs_ws, vo_att, 0, AUX_SC1);
                    if (!__any(empty(at))) break;
                }
        }
        #pragma unroll
        for (int i = 0; i < 4; ++i) lds_st<unsigned>(a_atw + 4 * i, at[i]);
        STAMP(13);
        TICK2(12);
        BARL();
        TICK2(13);
        STAMP(15);
        #pragma unroll
        for (int i = 0; i < 4; ++i) asm volatile("ds_read_u16_d16_hi %0, %1 offset:%2" : "+v"(avh[i]) : "v"(a_att0), "i"(i * 132) : "memory");
        #pragma unroll
        for (int i = 0; i < 4; ++i) asm volatile("ds_read_u16_d16_hi %0, %1 offset:%2" : "+v"(avh[4 + i]) : "v"(a_att1), "i"(i * 132) : "memory");
        f32x4 P0[8], P1[8];
        #pragma unroll
        for (int q = 0; q < 8; ++q) { P0[q] = lds_ld<f32x4>(ra[q]); P1[q] = lds_ld<f32x4>(rb[q]); }
        SB0();
        asm volatile("s_waitcnt lgkmcnt(15)" : "+v"(avh[0]), "+v"(avh[1]), "+v"(avh[2]), "+v"(avh[3]), "+v"(avh[4]), "+v"(avh[5]), "+v"(avh[6]), "+v"(avh[7]) :: "memory");
        unsigned ta[4];
        #pragma unroll
        for (int i = 0; i < 4; ++i) { const f32x2 t = xf[i] + f32x2{__uint_as_float(avh[2 * i]), __uint_as_float(avh[2 * i + 1])}; ta[i] = pk_bf(t.x, t.y); }
        const f32x4 s0 = ((P0[0] + P0[1]) + (P0[2] + P0[3])) + ((P0[4] + P0[5]) + (P0[6] + P0[7])), s1 = ((P1[0] + P1[1]) + (P1[2] + P1[3])) + ((P1[4] + P1[5]) + (P1[6] + P1[7]));
        unsigned ob[4];
        #pragma unroll
        for (int i = 0; i < 4; ++i) {
            const unsigned sb = i < 2 ? pk_bf(s0[2 * i], s0[2 * i + 1]) : pk_bf(s1[2 * i - 4], s1[2 * i - 3]);
            const f32x2 r = f32x2{bf2f(ta[i] & 0xFFFFu), __uint_as_float(ta[i] & 0xFFFF0000u)} + f32x2{bf2f(sb & 0xFFFFu), __uint_as_float(sb & 0xFFFF0000u)};
            ob[i] = pk_bf(r.x, r.y);
        }
        const u32x4 ov = {nbl ? ob[2] : ob[0], nbl ? ob[3] : ob[1], nbl ? ob[0] : ob[2], nbl ? ob[1] : ob[3]};
        __builtin_amdgcn_raw_buffer_store_b128(ov, mk_rsrc(p.out, L_ * D_ * 2), vo_out, 0, AUX_OUT);
        TICK2(16);
        if (v == TKM) TICK2_DUMP(17, 0);
    }
    STAMP(16); STAMPC(31);
    STAMP_DUMP();
}

extern "C" void kernel_launch(void* const* d_in, const int* in_sizes, int n_in, void* d_out, int out_size, void* d_ws, size_t ws_size, hipStream_t stream) {
    (void)in_sizes; (void)n_in; (void)out_size; (void)ws_size;
    Params p;
    p.x = (const u16*)d_in[0]; p.logA = (const u16*)d_in[1]; p.Aim = (const u16*)d_in[2]; p.Cre = (const u16*)d_in[3]; p.Cim = (const u16*)d_in[4];
    p.logdt = (const u16*)d_in[5]; p.Dskip = (const u16*)d_in[6]; p.wg = (const u16*)d_in[8]; p.wu = (const u16*)d_in[9]; p.wd = (const u16*)d_in[10];
    p.out = (u16*)d_out; p.ws = (unsigned char*)d_ws;
    (void)hipFuncSetAttribute((const void*)fused_kernel, hipFuncAttributeMaxDynamicSharedMemorySize, LDS_BYTES);
    fused_kernel<<<256, 768, LDS_BYTES, stream>>>(p);
}
```
